```python
import torch

def _softmax(x, axis=-1):
    return torch.softmax(x, dim=axis)
def _gelu(x):
    return torch.nn.functional.gelu(x, approximate="tanh")
def _silu(x):
    return torch.nn.functional.silu(x)
def _sigmoid(x):
    return torch.sigmoid(x)
def _layernorm(x, axis=-1, eps=1e-5):
    m = x.mean(dim=axis, keepdim=True)
    v = x.var(dim=axis, keepdim=True, unbiased=False)
    return (x - m) / torch.sqrt(v + eps)
def _scatter_add(tgt, idx, src, axis=0):
    return tgt.clone().index_add_(axis, idx, src)
def _segment_sum(src, seg, n):
    out = torch.zeros((n,) + src.shape[1:], dtype=src.dtype)
    return out.index_add_(0, seg, src)
def _pad(x, pw):
    flat = tuple(v for pair in reversed(pw) for v in pair)
    return torch.nn.functional.pad(x, flat)

kernel_name = 'gated_mix__linear_attn_block__mha_block__prompt'

def setup_inputs():

    _w = (lambda a: (lambda t: t.to(torch.bfloat16) if t.is_floating_point() else t)(torch.as_tensor(a)))
    def _A_setup():
        B, S, D = 1, 2048, 640
        return {
            "x": _w((lambda *s: torch.randn(*s, dtype=torch.bfloat16) * 0.05)(B, S, D)),
            "wqkv": _w((lambda *s: torch.randn(*s, dtype=torch.bfloat16) * 0.8)(D, 3 * D)),
            "wo": _w((lambda *s: torch.randn(*s, dtype=torch.bfloat16) * 0.2)(D, D)),
        }
    _A_in = _A_setup()

    def _B_setup():
        B, S, D, H = 1, 2048, 640, 32
        return {
            "x": _w((lambda *s: torch.randn(*s, dtype=torch.bfloat16) * 0.05)(B, S, D)),
            "wqkv": _w((lambda *s: torch.randn(*s, dtype=torch.bfloat16) * 0.8)(D, 3 * D)),
            "wo": _w((lambda *s: torch.randn(*s, dtype=torch.bfloat16) * 0.05)(D, D)),
        }
    _B_in = _B_setup()

    return {
        "A_x": _A_in["x"],
        "A_wqkv": _A_in["wqkv"],
        "A_wo": _A_in["wo"],
        "B_x": _B_in["x"],
        "B_wqkv": _B_in["wqkv"],
        "B_wo": _B_in["wo"],
        "gate_w": _w((lambda *s: torch.randn(*s, dtype=torch.bfloat16) * 0.05)(640) * 16.0),
    }

def reference(A_x, A_wqkv, A_wo, B_x, B_wqkv, B_wo, gate_w):
    def _A_ref(x, wqkv, wo):
        B, S, D = x.shape
        H = 32; Dh = D // H
        qkv = (lambda x, *d: x.permute(*d))((x @ wqkv).reshape(B, S, 3, H, Dh), 2, 0, 3, 1, 4)
        q, k, v = qkv[0], qkv[1], qkv[2]
        phi = lambda t: torch.where(t > 0, t + 1, torch.exp(t)) + 1e-6
        pq, pk = phi(q), phi(k)
        kv = torch.einsum("bhsd,bhse->bhde", pk, v)
        z = torch.einsum("bhsd->bhd", pk)
        num = torch.einsum("bhqd,bhde->bhqe", pq, kv)
        den = torch.einsum("bhqd,bhd->bhq", pq, z)[..., None] + 1e-6
        o = num / den
        return (lambda x, *d: x.permute(*d))(o, 0, 2, 1, 3).reshape(B, S, D) @ wo

    def _B_ref(x, wqkv, wo):
        B, S, D = x.shape
        H = 32
        Dh = D // H
        qkv = (lambda x, *d: x.permute(*d))((x @ wqkv).reshape(B, S, 3, H, Dh), 2, 0, 3, 1, 4)
        q, k, v = qkv[0], qkv[1], qkv[2]
        scores = torch.einsum("bhqd,bhkd->bhqk", q, k) * (Dh ** -0.5)
        p = _softmax(scores, axis=-1)
        o = torch.einsum("bhqk,bhkd->bhqd", p, v)
        return (lambda x, *d: x.permute(*d))(o, 0, 2, 1, 3).reshape(B, S, D) @ wo

    x = A_x
    g = _sigmoid((x * gate_w).sum(-1, keepdims=True))
    a = _A_ref(x, A_wqkv, A_wo)
    b = _B_ref(x, B_wqkv, B_wo)
    return g * a + (1 - g) * b
```

```cpp
#include <hip/hip_runtime.h>
#include <hip/hip_bf16.h>
#include <stdint.h>

typedef __attribute__((ext_vector_type(2))) __bf16 bf16x2;
typedef __attribute__((ext_vector_type(8))) __bf16 bf16x8;
typedef __attribute__((ext_vector_type(16))) __bf16 bf16x16;
typedef __attribute__((ext_vector_type(4))) short s16x4;
typedef __attribute__((ext_vector_type(2))) float f32x2;
typedef __attribute__((ext_vector_type(4))) float f32x4;
typedef __attribute__((ext_vector_type(8))) float f32x8;
typedef __attribute__((ext_vector_type(16))) float f32x16;
typedef __attribute__((ext_vector_type(2))) unsigned u32x2;
typedef __attribute__((ext_vector_type(4))) unsigned u32x4;
typedef __attribute__((address_space(3))) char* lds_ptr;

#define DI __device__ __forceinline__
#define SBAR() __builtin_amdgcn_sched_barrier(0)
#define PIN(x) asm volatile("" : "+v"(x))

constexpr int SEQ = 2048, DM = 640, NH = 32, DH = 20, N3 = 1920;
constexpr float QSCALE = 0.22360679774997896f * 1.4426950408889634f;

constexpr int KVT = 2624;
constexpr int QT_B = 1280;
constexpr size_t WS_KV = 0;
constexpr size_t WS_QB = WS_KV + (size_t)NH * 64 * KVT;
constexpr size_t WS_PQ = WS_QB + (size_t)NH * 64 * QT_B;
constexpr size_t WS_KVP = WS_PQ + (size_t)NH * 64 * QT_B;
constexpr size_t WS_KMAX = WS_KVP + (size_t)NH * 16 * 420 * 4;
constexpr size_t WS_G = WS_KMAX + (size_t)NH * 16 * 4;
constexpr size_t WS_O = WS_G + (size_t)SEQ * 4;
constexpr size_t WS_WO = WS_O + (size_t)128 * 40 * 1024;

DI unsigned rfl(unsigned v) { return __builtin_amdgcn_readfirstlane(v); }
DI unsigned cvtpk(float lo, float hi) {
  f32x2 v = {lo, hi};
  return __builtin_bit_cast(unsigned, __builtin_convertvector(v, bf16x2));
}
DI float bflo(unsigned u) { return __uint_as_float(u << 16); }
DI float bfhi(unsigned u) { return __uint_as_float(u & 0xffff0000u); }
DI float phi_f(float t) { return (t > 0.f ? t + 1.f : __expf(t)) + 1e-6f; }
DI void glds16(unsigned voff, const void* sbase, unsigned lds_base) {
  unsigned sv;
  asm volatile("s_mov_b32 %0, m0\n\ts_mov_b32 m0, %3\n\ts_nop 0\n\tglobal_load_lds_dwordx4 %1, %2\n\ts_mov_b32 m0, %0"
               : "=&s"(sv) : "v"(voff), "s"(sbase), "s"(lds_base) : "memory");
}
template <int N> DI void wait_bar() {
  asm volatile("s_waitcnt vmcnt(%0) lgkmcnt(0)\n\ts_barrier" ::"n"(N) : "memory");
}
DI void lds_bar() { asm volatile("s_waitcnt lgkmcnt(0)\n\ts_barrier" ::: "memory"); }
DI bf16x8 lds_r128(lds_ptr p) { return *(const __attribute__((address_space(3))) bf16x8*)p; }
DI s16x4 lds_r64(lds_ptr p) { return *(const __attribute__((address_space(3))) s16x4*)p; }
DI s16x4 lds_tr(lds_ptr p) {
  return __builtin_amdgcn_ds_read_tr16_b64_v4i16((__attribute__((address_space(3))) s16x4*)p);
}
DI bf16x8 cat8(s16x4 a, s16x4 b) {
  return __builtin_bit_cast(bf16x8, __builtin_shufflevector(a, b, 0, 1, 2, 3, 4, 5, 6, 7));
}
#define MFMA16(a, b, c) __builtin_amdgcn_mfma_f32_16x16x32_bf16(a, b, c, 0, 0, 0)
#define MFMA32(a, b, c) __builtin_amdgcn_mfma_f32_32x32x16_bf16(a, b, c, 0, 0, 0)
#define MFMA32K8(a, b, c) __builtin_amdgcn_mfma_f32_32x32x8bf16_1k(a, b, c, 0, 0, 0)
#define SMFMA32(a, b, c, i) __builtin_amdgcn_smfmac_f32_32x32x32_bf16(a, b, c, i, 0, 0)
DI bf16x16 lo8(const f32x16& p) {
  return __builtin_bit_cast(bf16x16, (f32x8)__builtin_shufflevector(p, p, 0, 1, 2, 3, 4, 5, 6, 7));
}
DI bf16x16 hi8(const f32x16& p) {
  return __builtin_bit_cast(bf16x16, (f32x8)__builtin_shufflevector(p, p, 8, 9, 10, 11, 12, 13, 14, 15));
}
DI float wave_sum(float v) {
#pragma unroll
  for (int o = 32; o > 0; o >>= 1) v += __shfl_xor(v, o);
  return v;
}
DI float wave_max(float v) {
#pragma unroll
  for (int o = 32; o > 0; o >>= 1) v = fmaxf(v, __shfl_xor(v, o));
  return v;
}
DI float swap32(float v) {
  auto r = __builtin_amdgcn_permlane32_swap(__float_as_uint(v), __float_as_uint(v), false, false);
  unsigned lane = __builtin_amdgcn_mbcnt_hi(~0u, __builtin_amdgcn_mbcnt_lo(~0u, 0u));
  return __uint_as_float(lane < 32 ? r[1] : r[0]);
}

constexpr int K1_STG = 23552;
constexpr int K1_NSLOT = 6;
constexpr int K1_LDS = K1_STG * K1_NSLOT;
constexpr int K1_XOFF = 15360;

template <int NHALF> struct TL {
  static constexpr int N = NHALF == 0 ? 8 : 7;
  static constexpr int sec(int i) { return i < 5 ? NHALF : 2; }
  static constexpr int t(int i) { return i < 5 ? i : (NHALF == 0 ? i - 5 : i - 2); }
};

struct K1Dma {
  unsigned voff[3];
  const char* base[3];
  unsigned inc[3];
  unsigned ldst[3];
};

template <int TYPE, int NHALF>
DI void k1_body(char* smem, const K1Dma& dm, char* ws, int rt, int g, int mw, int lane, int tid) {
  using T = TL<NHALF>;
  constexpr int NT = T::N;
  const lds_ptr lds = (lds_ptr)smem;
  const int g4 = lane >> 4, c16 = lane & 15;
  const lds_ptr wb0 = lds + (4 * g4 + (c16 >> 2)) * 160 + (lane & 3) * 8;
  const lds_ptr xb0 = lds + K1_XOFF + mw * 2048 + lane * 16;

  f32x4 acc[NT][2];
#pragma unroll
  for (int i = 0; i < NT; ++i) { acc[i][0] = f32x4{0, 0, 0, 0}; acc[i][1] = f32x4{0, 0, 0, 0}; }
  bf16x8 wf[2][NT], xf[2][2];

  auto issue = [&](int st) {
    const unsigned so = (unsigned)(st % K1_NSLOT) * K1_STG;
#pragma unroll
    for (int j = 0; j < 3; ++j) glds16(dm.voff[j], dm.base[j] + (size_t)st * dm.inc[j], dm.ldst[j] + so);
  };
#pragma unroll
  for (int st = 0; st < K1_NSLOT; ++st) issue(st);
  wait_bar<15>();
  {
    xf[0][0] = lds_r128(xb0); xf[0][1] = lds_r128(xb0 + 1024);
#pragma unroll
    for (int i = 0; i < NT; ++i) {
      const lds_ptr p = wb0 + T::sec(i) * 5120 + T::t(i) * 32;
      wf[0][i] = cat8(lds_tr(p), lds_tr(p + 2560));
    }
  }
#pragma unroll
  for (int p = 0; p < 20; ++p) {
    const int cur = p & 1, nxt = cur ^ 1;
    if (p < 19) {
      if (p <= 14) wait_bar<12>();
      else if (p == 15) wait_bar<9>();
      else if (p == 16) wait_bar<6>();
      else if (p == 17) wait_bar<3>();
      else wait_bar<0>();
      if (p + K1_NSLOT < 20) issue(p + K1_NSLOT);
    }
    const int so = ((p + 1) % K1_NSLOT) * K1_STG;
    const lds_ptr wb = wb0 + so, xb = xb0 + so;
    if (p < 19) { xf[nxt][0] = lds_r128(xb); xf[nxt][1] = lds_r128(xb + 1024); }
#pragma unroll
    for (int i = 0; i < NT; ++i) {
      if (TYPE == 0) {
        acc[i][0] = MFMA16(xf[cur][0], wf[cur][i], acc[i][0]);
        acc[i][1] = MFMA16(xf[cur][1], wf[cur][i], acc[i][1]);
      } else {
        acc[i][0] = MFMA16(wf[cur][i], xf[cur][0], acc[i][0]);
        acc[i][1] = MFMA16(wf[cur][i], xf[cur][1], acc[i][1]);
      }
      if (p < 19) {
        const lds_ptr q = wb + T::sec(i) * 5120 + T::t(i) * 32;
        wf[nxt][i] = cat8(lds_tr(q), lds_tr(q + 2560));
      }
      SBAR();
    }
  }
  lds_bar();

  const int kt = rt * 4 + mw;
  if (TYPE == 1) {
    float* nrm = (float*)smem;
#pragma unroll
    for (int i = 0; i < NT; ++i) {
      const int sec = T::sec(i);
      const int ng = 4 * T::t(i) + g4, hh = ng / 5, dg = ng - hh * 5, head = g * 4 + hh;
#pragma unroll
      for (int mt = 0; mt < 2; ++mt) {
        const int r = 16 * mt + c16;
        const int off = dg < 4 ? ((dg >> 1) * 32 + r) * 16 + (dg & 1) * 8 : 1024 + r * 8;
        f32x4 v = acc[i][mt];
        u32x2 o;
        if (sec == 0) {
          unsigned a = cvtpk(v[0], v[1]), b = cvtpk(v[2], v[3]);
          o[0] = cvtpk(phi_f(bflo(a)), phi_f(bfhi(a)));
          o[1] = cvtpk(phi_f(bflo(b)), phi_f(bfhi(b)));
          *(u32x2*)(ws + WS_PQ + (size_t)(head * 64 + kt) * QT_B + off) = o;
        } else if (sec == 1) {
          o[0] = cvtpk(v[0] * QSCALE, v[1] * QSCALE);
          o[1] = cvtpk(v[2] * QSCALE, v[3] * QSCALE);
          *(u32x2*)(ws + WS_QB + (size_t)(head * 64 + kt) * QT_B + off) = o;
        } else {
          o[0] = cvtpk(v[0], v[1]); o[1] = cvtpk(v[2], v[3]);
          *(u32x2*)(ws + WS_KV + (size_t)(head * 64 + kt) * KVT + off) = o;
          float a0 = bflo(o[0]), a1 = bfhi(o[0]), a2 = bflo(o[1]), a3 = bfhi(o[1]);
          nrm[(mw * 32 + r) * 21 + ng] = a0 * a0 + a1 * a1 + a2 * a2 + a3 * a3;
        }
      }
    }
    lds_bar();
    {
      const int hh = tid >> 7, tok = tid & 127;
      const float* p = nrm + tok * 21 + hh * 5;
      float s = (p[0] + p[1]) + (p[2] + p[3]) + p[4];
      s = (s < 3.0e38f) ? s : __builtin_inff();
      s = wave_max(s);
      float* red = nrm + 128 * 21;
      if (lane == 0) red[tid >> 6] = s;
      lds_bar();
      if (tid < 4) *(float*)(ws + WS_KMAX + (size_t)((g * 4 + tid) * 16 + rt) * 4) = fmaxf(red[2 * tid], red[2 * tid + 1]);
    }
  } else {
#pragma unroll
    for (int i = 0; i < NT; ++i) {
      const int sec = T::sec(i);
      f32x4 v0 = acc[i][0], v1 = acc[i][1];
      u32x4 o = {cvtpk(v0[0], v0[1]), cvtpk(v0[2], v0[3]), cvtpk(v1[0], v1[1]), cvtpk(v1[2], v1[3])};
      if (sec == 0) {
#pragma unroll
        for (int j = 0; j < 4; ++j) o[j] = cvtpk(phi_f(bflo(o[j])), phi_f(bfhi(o[j])));
      }
      if (sec < 2) {
        *(u32x4*)(smem + ((sec * 5 + T::t(i)) * 4 + mw) * 1024 + lane * 16) = o;
      } else {
        const int n = 16 * T::t(i) + c16, hh = n / 20, dv = n - hh * 20, head = g * 4 + hh;
        char* dst = ws + WS_KV + (size_t)(head * 64 + kt) * KVT + 1280 + (g4 >> 1) * 336 + dv * 16 + (g4 & 1) * 8;
        *(u32x2*)dst = u32x2{o[0], o[1]};
        *(u32x2*)(dst + 672) = u32x2{o[2], o[3]};
      }
    }
    if (NHALF == 1 && lane < 16) {
      const int hh = lane >> 2, blk = lane & 3;
      char* dst = ws + WS_KV + (size_t)((g * 4 + hh) * 64 + kt) * KVT + 1280 + blk * 336 + 320;
      *(u32x4*)dst = u32x4{0x3f803f80u, 0x3f803f80u, 0x3f803f80u, 0x3f803f80u};
    }
    lds_bar();
    const int w = tid >> 6;
    for (int job = w; job < 18; job += 8) {
      int ti, tj;
      if (job < 13) { ti = (job + 1) / 3; const int m = (job + 1) - ti * 3; tj = ti + m - 1; }
      else { ti = job - 13; tj = -1; }
      f32x4 d = {0, 0, 0, 0};
#pragma unroll
      for (int m4 = 0; m4 < 4; ++m4) {
        bf16x8 a = lds_r128(lds + (ti * 4 + m4) * 1024 + lane * 16);
        bf16x8 b;
        if (tj >= 0) b = lds_r128(lds + ((5 + tj) * 4 + m4) * 1024 + lane * 16);
        else b = __builtin_bit_cast(bf16x8, u32x4{0x3f803f80u, 0x3f803f80u, 0x3f803f80u, 0x3f803f80u});
        d = MFMA16(a, b, d);
      }
      const int dd = 16 * ti + 4 * g4, hd = dd / 20;
      float* dst = (float*)(ws + WS_KVP) + (size_t)((g * 4 + hd) * 16 + rt) * 420;
      if (tj >= 0) {
        const int e = 16 * tj + c16, he = e / 20;
        if (he == hd) *(f32x4*)(dst + (e - he * 20) * 20 + (dd - hd * 20)) = d;
      } else if (c16 == 0) {
        *(f32x4*)(dst + 400 + (dd - hd * 20)) = d;
      }
    }
  }
}

__global__ __launch_bounds__(512, 2) void k1_qkv(const __hip_bfloat16* __restrict__ x, const __hip_bfloat16* __restrict__ wa,
                                                 const __hip_bfloat16* __restrict__ wb, const __hip_bfloat16* __restrict__ woa,
                                                 const __hip_bfloat16* __restrict__ wob, const __hip_bfloat16* __restrict__ gw,
                                                 char* __restrict__ ws) {
  extern __shared__ __attribute__((aligned(16))) char smem[];
  const int tid = threadIdx.x, lane = tid & 63, w = rfl(tid >> 6);
  const int b = blockIdx.x, xcd = b & 7, bi = b >> 3;
  const int rt = (xcd & 1) * 8 + (bi & 7), ct = (xcd >> 1) * 4 + (bi >> 3);
  const int type = ct & 1, g = ct >> 1;

  {
    const int row = b * 8 + w;
    const unsigned* xr = (const unsigned*)(x + (size_t)row * DM);
    const unsigned* gr = (const unsigned*)gw;
    float s = 0.f;
#pragma unroll
    for (int j = 0; j < 5; ++j) {
      unsigned a = xr[lane + 64 * j], c = gr[lane + 64 * j];
      s += bflo(a) * bflo(c) + bfhi(a) * bfhi(c);
    }
    s = wave_sum(s);
    if (lane == 0) *(float*)(ws + WS_G + (size_t)row * 4) = 1.f / (1.f + __expf(-s));
  }
  {
    const int f = b * 8 + w;
    if (f < 1600) {
      const int nt = f / 40, ks = f - nt * 40;
      const int k0 = 32 * ks + 8 * (lane >> 4), n = 16 * nt + (lane & 15);
      const unsigned short* src = (const unsigned short*)(k0 < DM ? woa + (size_t)k0 * DM : wob + (size_t)(k0 - DM) * DM) + n;
      u32x4 o;
#pragma unroll
      for (int j = 0; j < 4; ++j) o[j] = (unsigned)src[(2 * j) * DM] | ((unsigned)src[(2 * j + 1) * DM] << 16);
      *(u32x4*)(ws + WS_WO + (size_t)f * 1024 + lane * 16) = o;
    }
  }
  asm volatile("s_waitcnt vmcnt(0)" ::: "memory");

  K1Dma dm;
  const unsigned lds0 = (unsigned)(size_t)smem;
  const char* s0 = (const char*)(type ? wa + 80 * g : wa + 640 + 80 * g);
  const char* s1 = (const char*)(type ? wb + 80 * g : wa + 1280 + 80 * g);
  const char* s2 = (const char*)(type ? wb + 640 + 80 * g : wb + 1280 + 80 * g);
#pragma unroll
  for (int i = 0; i < 3; ++i) {
    int j = w + 8 * i;
    if (j > 22) j = 22;
    if (j < 15) {
      const int sec = j / 5, pb = j - sec * 5;
      const int c = pb * 64 + lane, rho = c / 10, ci = c - rho * 10;
      const int k = 8 * ((rho & 15) >> 2) + 4 * (rho >> 4) + (rho & 3);
      dm.voff[i] = (unsigned)(k * (N3 * 2) + ci * 16);
      dm.base[i] = sec == 0 ? s0 : (sec == 1 ? s1 : s2);
      dm.inc[i] = 32 * N3 * 2;
    } else {
      const int xb = j - 15;
      dm.voff[i] = (unsigned)((rt * 128 + xb * 16 + (lane & 15)) * (DM * 2) + (lane >> 4) * 16);
      dm.base[i] = (const char*)x;
      dm.inc[i] = 64;
    }
    dm.ldst[i] = lds0 + j * 1024;
  }
  const int mw = w & 3;
  if (type) {
    if (w < 4) k1_body<1, 0>(smem, dm, ws, rt, g, mw, lane, tid);
    else k1_body<1, 1>(smem, dm, ws, rt, g, mw, lane, tid);
  } else {
    if (w < 4) k1_body<0, 0>(smem, dm, ws, rt, g, mw, lane, tid);
    else k1_body<0, 1>(smem, dm, ws, rt, g, mw, lane, tid);
  }
}

constexpr int K2_STG = 16 * KVT;
constexpr int K2_RING = 3 * K2_STG;
constexpr int K2_KVF = K2_RING;
constexpr int K2_OX = K2_KVF + 1536;
constexpr int K2_MX = K2_OX + 24576;
constexpr int K2_LDS = K2_MX + 4096 + 16;

#define EXP4(P, S, i)                                                                     \
  P[i] = __builtin_amdgcn_exp2f(S[i]); P[i + 1] = __builtin_amdgcn_exp2f(S[i + 1]);       \
  P[i + 2] = __builtin_amdgcn_exp2f(S[i + 2]); P[i + 3] = __builtin_amdgcn_exp2f(S[i + 3]); \
  PIN(P); SBAR();

DI void store_o(char* ws, const f32x16& o, float sc, int tok0, int kbase, int lane) {
  const int r = lane & 31, hh = lane >> 5, tok = tok0 + r;
  char* base = ws + WS_O + (size_t)(tok >> 4) * 40960 + (tok & 15) * 16;
#pragma unroll
  for (int q = 0; q < 3; ++q) {
    const int dv = 8 * q + 4 * hh;
    if (dv < DH) {
      const int k = kbase + dv;
      u32x2 v = {cvtpk(o[4 * q] * sc, o[4 * q + 1] * sc), cvtpk(o[4 * q + 2] * sc, o[4 * q + 3] * sc)};
      *(u32x2*)(base + (k >> 5) * 1024 + ((k >> 3) & 3) * 256 + (k & 7) * 2) = v;
    }
  }
}

__global__ __launch_bounds__(512, 2) void k2_attn(char* __restrict__ ws) {
  extern __shared__ __attribute__((aligned(16))) char smem[];
  const lds_ptr lds = (lds_ptr)smem;
  const int tid = threadIdx.x, lane = tid & 63, w = rfl(tid >> 6);
  const int b = blockIdx.x, xcd = b & 7, bi = b >> 3;
  const int h = xcd * 4 + (bi & 3), qb = bi >> 2;
  const int qs = w & 3, kh = w >> 2;
  const int r32 = lane & 31, hh = lane >> 5;
  const int qt0 = qb * 8 + qs * 2;
  const char* kvg = ws + WS_KV + (size_t)h * 64 * KVT;
  const unsigned lds0 = (unsigned)(size_t)smem;

  bf16x8 qa0, qb0; s16x4 qa1, qb1;
  {
    const char* qp = ws + WS_QB + (size_t)(h * 64 + qt0) * QT_B;
    qa0 = *(const bf16x8*)(qp + lane * 16);
    qb0 = *(const bf16x8*)(qp + QT_B + lane * 16);
    u32x2 a = *(const u32x2*)(qp + 1024 + r32 * 8), c = *(const u32x2*)(qp + QT_B + 1024 + r32 * 8);
    if (hh) { a = u32x2{0, 0}; c = u32x2{0, 0}; }
    qa1 = __builtin_bit_cast(s16x4, a); qb1 = __builtin_bit_cast(s16x4, c);
  }
  float kmax2;
  {
    kmax2 = *(const float*)(ws + WS_KMAX + (size_t)(h * 16 + (lane & 15)) * 4);
#pragma unroll
    for (int o = 8; o > 0; o >>= 1) kmax2 = fmaxf(kmax2, __shfl_xor(kmax2, o));
  }
  {
    float s = 0.f;
    if (tid < 420) {
      const float* p = (const float*)(ws + WS_KVP) + (size_t)h * 16 * 420 + tid;
#pragma unroll
      for (int i = 0; i < 16; ++i) s += p[i * 420];
    }
    if (tid < 384) *(unsigned*)(smem + K2_KVF + tid * 4) = 0u;
    lds_bar();
    if (tid < 420) {
      const int e = tid / 20, d = tid - e * 20;
      const int off = d < 16 ? ((d >> 3) * 32 + e) * 16 + (d & 7) * 2 : 1024 + e * 8 + (d - 16) * 2;
      *(unsigned short*)(smem + K2_KVF + off) = (unsigned short)(cvtpk(s, s) & 0xffffu);
    }
  }
  float ma, mb;
  {
    u32x4 a = __builtin_bit_cast(u32x4, qa0), c = __builtin_bit_cast(u32x4, qb0);
    u32x2 a1 = __builtin_bit_cast(u32x2, qa1), c1 = __builtin_bit_cast(u32x2, qb1);
    float na = 0.f, nb = 0.f;
#pragma unroll
    for (int j = 0; j < 4; ++j) {
      na += bflo(a[j]) * bflo(a[j]) + bfhi(a[j]) * bfhi(a[j]);
      nb += bflo(c[j]) * bflo(c[j]) + bfhi(c[j]) * bfhi(c[j]);
    }
#pragma unroll
    for (int j = 0; j < 2; ++j) {
      na += bflo(a1[j]) * bflo(a1[j]) + bfhi(a1[j]) * bfhi(a1[j]);
      nb += bflo(c1[j]) * bflo(c1[j]) + bfhi(c1[j]) * bfhi(c1[j]);
    }
    na += swap32(na); nb += swap32(nb);
    ma = sqrtf(na * kmax2); mb = sqrtf(nb * kmax2);
  }
  asm volatile("s_waitcnt vmcnt(0)" ::: "memory");

  const int vr = r32 <= 20 ? r32 : (r32 >= 28 ? 4 : 20);
  const lds_ptr k0b = lds + kh * 8 * KVT + lane * 16;
  const lds_ptr k1b = lds + kh * 8 * KVT + 1024 + r32 * 8;
  const lds_ptr vb = lds + kh * 8 * KVT + 1280 + hh * 336 + vr * 16;
  int idx = 0xDDDD; PIN(idx);
  const unsigned dvoff = (unsigned)(w * 5120 + lane * 16);

  f32x16 oa, ob;
  for (int attempt = 0;; ++attempt) {
    if (attempt == 1) {
      float xa = -__builtin_inff(), xb = -__builtin_inff();
      const f32x16 z = {};
      for (int t = 0; t < 32; ++t) {
        const char* kp = kvg + (size_t)((t >> 3) * 16 + kh * 8 + (t & 7)) * KVT;
        bf16x8 k0 = *(const bf16x8*)(kp + lane * 16);
        s16x4 k1 = *(const s16x4*)(kp + 1024 + r32 * 8);
        f32x16 sa = MFMA32(k0, qa0, z); sa = MFMA32K8(k1, qa1, sa);
        f32x16 sb = MFMA32(k0, qb0, z); sb = MFMA32K8(k1, qb1, sb);
#pragma unroll
        for (int i = 0; i < 16; ++i) { xa = fmaxf(xa, sa[i]); xb = fmaxf(xb, sb[i]); }
      }
      xa = fmaxf(xa, swap32(xa)); xb = fmaxf(xb, swap32(xb));
      float* mx = (float*)(smem + K2_MX);
      mx[(w * 2 + 0) * 64 + lane] = xa; mx[(w * 2 + 1) * 64 + lane] = xb;
      lds_bar();
      ma = fmaxf(xa, mx[((w ^ 4) * 2 + 0) * 64 + lane]);
      mb = fmaxf(xb, mx[((w ^ 4) * 2 + 1) * 64 + lane]);
      asm volatile("s_waitcnt vmcnt(0)" ::: "memory");
    }
    f32x16 nma, nmb;
#pragma unroll
    for (int i = 0; i < 16; ++i) { nma[i] = -ma; nmb[i] = -mb; oa[i] = 0.f; ob[i] = 0.f; }
    PIN(nma); PIN(nmb);

    auto issue = [&](int st) {
      const char* src = kvg + (size_t)st * K2_STG;
      const unsigned dst = lds0 + (unsigned)(st % 3) * K2_STG;
#pragma unroll
      for (int j = 0; j < 5; ++j) glds16(dvoff + j * 1024, src, dst + w * 5120 + j * 1024);
    };
    auto issue_x = [&](int st) {
      glds16((unsigned)(40960 + lane * 16), kvg + (size_t)st * K2_STG, lds0 + (unsigned)(st % 3) * K2_STG + 40960);
    };
    if (w == 0) { issue_x(0); issue_x(1); issue_x(2); }
    issue(0); issue(1); issue(2);
    wait_bar<10>();

    f32x16 sa, sb, pa, pb;
    bf16x8 k0c, k0n, v0c, v1c, v0p, v1p; s16x4 k1c, k1n;
    k0c = lds_r128(k0b); k1c = lds_r64(k1b);
    sa = MFMA32(k0c, qa0, nma); sa = MFMA32K8(k1c, qa1, sa);
#pragma unroll
    for (int i = 0; i < 16; ++i) pb[i] = 0.f;
    v0p = k0c; v1p = k0c;

    for (int st = 0; st < 4; ++st) {
      const int so = (st % 3) * K2_STG, sn = ((st + 1) % 3) * K2_STG;
#pragma unroll
      for (int tt = 0; tt < 8; ++tt) {
        if (tt == 7) {
          if (st == 0) wait_bar<5>();
          else if (st == 1) { wait_bar<0>(); if (w == 0) issue_x(3); issue(3); }
          else if (st == 2) wait_bar<0>();
        }
        {
          const int ko = tt < 7 ? so + (tt + 1) * KVT : (st < 3 ? sn : so);
          k0n = lds_r128(k0b + ko); k1n = lds_r64(k1b + ko);
          v0c = lds_r128(vb + so + tt * KVT); v1c = lds_r128(vb + so + tt * KVT + 672);
        }
        SBAR();
        sb = MFMA32(k0c, qb0, nmb);            EXP4(pa, sa, 0)
        sb = MFMA32K8(k1c, qb1, sb);           EXP4(pa, sa, 4)
        ob = SMFMA32(v0p, lo8(pb), ob, idx);   EXP4(pa, sa, 8)
        ob = SMFMA32(v1p, hi8(pb), ob, idx);   EXP4(pa, sa, 12)
        sa = MFMA32(k0n, qa0, nma);            EXP4(pb, sb, 0)
        sa = MFMA32K8(k1n, qa1, sa);           EXP4(pb, sb, 4)
        oa = SMFMA32(v0c, lo8(pa), oa, idx);   EXP4(pb, sb, 8)
        oa = SMFMA32(v1c, hi8(pa), oa, idx);   EXP4(pb, sb, 12)
        k0c = k0n; k1c = k1n; v0p = v0c; v1p = v1c;
      }
    }
    ob = SMFMA32(v0p, lo8(pb), ob, idx);
    ob = SMFMA32(v1p, hi8(pb), ob, idx);

    float* ox = (float*)(smem + K2_OX) + qs * (2 * 12 * 64);
    int* flag = (int*)(smem + K2_MX + 4096);
    if (tid == 0) *flag = 0;
    if (kh == 1) {
#pragma unroll
      for (int i = 0; i < 12; ++i) { ox[i * 64 + lane] = oa[i]; ox[(12 + i) * 64 + lane] = ob[i]; }
    }
    lds_bar();
    if (kh == 0) {
#pragma unroll
      for (int i = 0; i < 12; ++i) { oa[i] += ox[i * 64 + lane]; ob[i] += ox[(12 + i) * 64 + lane]; }
      const bool bad = hh && !(oa[8] >= 5.9604645e-8f && oa[8] < 3.0e38f && ob[8] >= 5.9604645e-8f && ob[8] < 3.0e38f);
      if (__any(bad) && lane == 0) *flag = 1;
    }
    lds_bar();
    const int redo = *flag;
    if (!redo || attempt == 1) break;
    lds_bar();
  }

  const int tok0 = qt0 * 32;
  const float* gp = (const float*)(ws + WS_G) + tok0 + r32;
  if (kh == 0) {
    float la = swap32(oa[8]), lb = swap32(ob[8]);
    if (hh) { la = oa[8]; lb = ob[8]; }
    store_o(ws, oa, (1.f - gp[0]) / la, tok0, DM + h * DH, lane);
    store_o(ws, ob, (1.f - gp[32]) / lb, tok0 + 32, DM + h * DH, lane);
  } else {
    const char* pp = ws + WS_PQ + (size_t)(h * 64 + qt0) * QT_B;
    bf16x8 pa0 = *(const bf16x8*)(pp + lane * 16), pb0 = *(const bf16x8*)(pp + QT_B + lane * 16);
    u32x2 a = *(const u32x2*)(pp + 1024 + r32 * 8), c = *(const u32x2*)(pp + QT_B + 1024 + r32 * 8);
    if (hh) { a = u32x2{0, 0}; c = u32x2{0, 0}; }
    bf16x8 f0 = lds_r128(lds + K2_KVF + lane * 16);
    s16x4 f1 = lds_r64(lds + K2_KVF + 1024 + lane * 8);
    const f32x16 z = {};
    f32x16 da = MFMA32(f0, pa0, z); da = MFMA32K8(f1, __builtin_bit_cast(s16x4, a), da);
    f32x16 db = MFMA32(f0, pb0, z); db = MFMA32K8(f1, __builtin_bit_cast(s16x4, c), db);
    float la = swap32(da[8]), lb = swap32(db[8]);
    if (hh) { la = da[8]; lb = db[8]; }
    store_o(ws, da, gp[0] / (la + 1e-6f), tok0, h * DH, lane);
    store_o(ws, db, gp[32] / (lb + 1e-6f), tok0 + 32, h * DH, lane);
  }
}

constexpr int K3_RS = 336;
constexpr int K3_LDS = 4 * 64 * K3_RS;

__global__ __launch_bounds__(256, 1) void k3_out(const char* __restrict__ ws, __hip_bfloat16* __restrict__ out) {
  extern __shared__ __attribute__((aligned(16))) char smem[];
  const int tid = threadIdx.x, lane = tid & 63, w = rfl(tid >> 6);
  const int b = blockIdx.x, xcd = b & 7, bi = b >> 3;
  const int rt = (xcd & 3) * 8 + (bi & 7), nt = (xcd >> 2) * 4 + (bi >> 3);
  const char* op = ws + WS_O + (size_t)(rt * 4) * 40960 + (size_t)(w * 10) * 1024 + lane * 16;
  const char* wp = ws + WS_WO + (size_t)(nt * 5) * 40960 + (size_t)(w * 10) * 1024 + lane * 16;
  bf16x8 of[10][4], wf[10][5];
#pragma unroll
  for (int ks = 0; ks < 10; ++ks) {
#pragma unroll
    for (int m = 0; m < 4; ++m) of[ks][m] = *(const bf16x8*)(op + m * 40960 + ks * 1024);
#pragma unroll
    for (int n = 0; n < 5; ++n) wf[ks][n] = *(const bf16x8*)(wp + n * 40960 + ks * 1024);
  }
  SBAR();
  f32x4 acc[4][5];
#pragma unroll
  for (int m = 0; m < 4; ++m)
#pragma unroll
    for (int n = 0; n < 5; ++n) acc[m][n] = f32x4{0, 0, 0, 0};
#pragma unroll
  for (int ks = 0; ks < 10; ++ks)
#pragma unroll
    for (int m = 0; m < 4; ++m)
#pragma unroll
      for (int n = 0; n < 5; ++n) acc[m][n] = MFMA16(wf[ks][n], of[ks][m], acc[m][n]);
#pragma unroll
  for (int m = 0; m < 4; ++m)
#pragma unroll
    for (int n = 0; n < 5; ++n)
      *(f32x4*)(smem + (w * 64 + 16 * m + (lane & 15)) * K3_RS + (16 * n + 4 * (lane >> 4)) * 4) = acc[m][n];
  __syncthreads();
  for (int job = tid; job < 640; job += 256) {
    const int row = job / 10, ch = job - row * 10;
    f32x4 s0 = {0, 0, 0, 0}, s1 = {0, 0, 0, 0};
#pragma unroll
    for (int ww = 0; ww < 4; ++ww) {
      const char* p = smem + (ww * 64 + row) * K3_RS + ch * 32;
      s0 += *(const f32x4*)p; s1 += *(const f32x4*)(p + 16);
    }
    u32x4 o = {cvtpk(s0[0], s0[1]), cvtpk(s0[2], s0[3]), cvtpk(s1[0], s1[1]), cvtpk(s1[2], s1[3])};
    *(u32x4*)((char*)out + (size_t)(rt * 64 + row) * (DM * 2) + nt * 160 + ch * 16) = o;
  }
}

extern "C" void kernel_launch(void* const* d_in, const int* in_sizes, int n_in, void* d_out, int out_size,
                              void* d_ws, size_t ws_size, hipStream_t stream) {
  const __hip_bfloat16* x = (const __hip_bfloat16*)d_in[0];
  const __hip_bfloat16* wa = (const __hip_bfloat16*)d_in[1];
  const __hip_bfloat16* woa = (const __hip_bfloat16*)d_in[2];
  const __hip_bfloat16* wb = (const __hip_bfloat16*)d_in[4];
  const __hip_bfloat16* wob = (const __hip_bfloat16*)d_in[5];
  const __hip_bfloat16* gw = (const __hip_bfloat16*)d_in[6];
  char* ws = (char*)d_ws;
  (void)hipFuncSetAttribute((const void*)k1_qkv, hipFuncAttributeMaxDynamicSharedMemorySize, K1_LDS);
  (void)hipFuncSetAttribute((const void*)k2_attn, hipFuncAttributeMaxDynamicSharedMemorySize, K2_LDS);
  (void)hipFuncSetAttribute((const void*)k3_out, hipFuncAttributeMaxDynamicSharedMemorySize, K3_LDS);
  k1_qkv<<<256, 512, K1_LDS, stream>>>(x, wa, wb, woa, wob, gw, ws);
  k2_attn<<<256, 512, K2_LDS, stream>>>(ws);
  k3_out<<<256, 256, K3_LDS, stream>>>(ws, (__hip_bfloat16*)d_out);
}
```

```cpp
#include <hip/hip_runtime.h>
#include <hip/hip_bf16.h>
#include <stdint.h>

typedef __attribute__((ext_vector_type(2))) __bf16 bf16x2;
typedef __attribute__((ext_vector_type(8))) __bf16 bf16x8;
typedef __attribute__((ext_vector_type(16))) __bf16 bf16x16;
typedef __attribute__((ext_vector_type(4))) short s16x4;
typedef __attribute__((ext_vector_type(2))) float f32x2;
typedef __attribute__((ext_vector_type(4))) float f32x4;
typedef __attribute__((ext_vector_type(8))) float f32x8;
typedef __attribute__((ext_vector_type(16))) float f32x16;
typedef __attribute__((ext_vector_type(2))) unsigned u32x2;
typedef __attribute__((ext_vector_type(4))) unsigned u32x4;
typedef __attribute__((address_space(3))) char* lds_ptr;

#define DI __device__ __forceinline__
#define SBAR() __builtin_amdgcn_sched_barrier(0)
#define PIN(x) asm volatile("" : "+v"(x))

constexpr int SEQ = 2048, DM = 640, NH = 32, DH = 20, N3 = 1920;
constexpr float QSCALE = 0.22360679774997896f * 1.4426950408889634f;

constexpr int REC = 10240, QREC = 5120;
constexpr size_t WS_KV = 0;
constexpr size_t WS_QB = WS_KV + (size_t)8 * 64 * REC;
constexpr size_t WS_PQ = WS_QB + (size_t)8 * 64 * QREC;
constexpr size_t WS_KVP = WS_PQ + (size_t)8 * 64 * QREC;
constexpr size_t WS_KMAX = WS_KVP + (size_t)16 * 16 * 4096;
constexpr size_t WS_G = WS_KMAX + (size_t)160 * 32 * 4;
constexpr size_t WS_O = WS_G + (size_t)SEQ * 4;
constexpr size_t WS_WO = WS_O + (size_t)128 * 40 * 1024;
constexpr size_t WS_CA = WS_WO + (size_t)40 * 40 * 1024;
constexpr size_t WS_CB = WS_CA + 256 * 256;
constexpr size_t WS_ST = WS_CB + 256 * 256;
constexpr size_t WS_CN = WS_ST + 1024;

#define WS_SHIFT 0
DI unsigned rfl(unsigned v) { return __builtin_amdgcn_readfirstlane(v); }
DI unsigned cvtpk(float lo, float hi) {
  f32x2 v = {lo, hi};
  return __builtin_bit_cast(unsigned, __builtin_convertvector(v, bf16x2));
}
DI float bflo(unsigned u) { return __uint_as_float(u << 16); }
DI float bfhi(unsigned u) { return __uint_as_float(u & 0xffff0000u); }
DI void glds16(unsigned voff, const void* sbase, unsigned lds_base) {
  asm volatile("s_mov_b32 m0, %2\n\ts_nop 0\n\tglobal_load_lds_dwordx4 %0, %1"
               :: "v"(voff), "s"(sbase), "s"(lds_base) : "memory", "m0");
}
DI void lds_bar() { asm volatile("s_waitcnt lgkmcnt(0)\n\ts_barrier" ::: "memory"); }
DI bf16x8 lds_r128(lds_ptr p) { return *(const __attribute__((address_space(3))) bf16x8*)p; }
DI s16x4 lds_r64(lds_ptr p) { return *(const __attribute__((address_space(3))) s16x4*)p; }
DI s16x4 lds_tr(lds_ptr p) {
  return __builtin_amdgcn_ds_read_tr16_b64_v4i16((__attribute__((address_space(3))) s16x4*)p);
}
DI bf16x8 cat8(s16x4 a, s16x4 b) {
  return __builtin_bit_cast(bf16x8, __builtin_shufflevector(a, b, 0, 1, 2, 3, 4, 5, 6, 7));
}
#define MFMA16(a, b, c) __builtin_amdgcn_mfma_f32_16x16x32_bf16(a, b, c, 0, 0, 0)
#define MFMA32(a, b, c) __builtin_amdgcn_mfma_f32_32x32x16_bf16(a, b, c, 0, 0, 0)
#define MFMA32K8(a, b, c) __builtin_amdgcn_mfma_f32_32x32x8bf16_1k(a, b, c, 0, 0, 0)
#define SMFMA32(a, b, c, i) __builtin_amdgcn_smfmac_f32_32x32x32_bf16(a, b, c, i, 0, 0)
DI bf16x16 lo8(const f32x16& p) {
  return __builtin_bit_cast(bf16x16, (f32x8)__builtin_shufflevector(p, p, 0, 1, 2, 3, 4, 5, 6, 7));
}
DI bf16x16 hi8(const f32x16& p) {
  return __builtin_bit_cast(bf16x16, (f32x8)__builtin_shufflevector(p, p, 8, 9, 10, 11, 12, 13, 14, 15));
}
DI float swap32(float v) {
  auto r = __builtin_amdgcn_permlane32_swap(__float_as_uint(v), __float_as_uint(v), false, false);
  unsigned lane = __builtin_amdgcn_mbcnt_hi(~0u, __builtin_amdgcn_mbcnt_lo(~0u, 0u));
  return __uint_as_float(lane < 32 ? r[1] : r[0]);
}

typedef volatile __attribute__((address_space(3))) int* lds_vint;
#define LP(a) ((lds_ptr)(size_t)(a))
DI int lds_ld(lds_ptr p) { return *(lds_vint)p; }
DI void lds_st(lds_ptr p, int v) { *(lds_vint)p = v; }
DI void wait_ge(int& v, int need, unsigned addr) {
  int t;
  asm volatile("v_readfirstlane_b32 %1, %0\n\ts_cmp_ge_i32 %1, %2\n\ts_cbranch_scc0 0f\n1:\n"
               ".subsection 1\n"
               "0:\n\ts_sleep 1\n\tds_read_b32 %0, %3\n\ts_waitcnt lgkmcnt(0)\n\t"
               "v_readfirstlane_b32 %1, %0\n\ts_cmp_ge_i32 %1, %2\n\ts_cbranch_scc0 0b\n\ts_branch 1b\n"
               ".subsection 0"
               : "+v"(v), "=&s"(t) : "s"(need), "v"(addr) : "memory", "scc");
}
DI void wait_ge2(int t, int need, unsigned addr) {
  int v;
  asm volatile("s_cmp_ge_i32 %1, %2\n\ts_cbranch_scc0 0f\n1:\n"
               ".subsection 1\n"
               "0:\n\tds_read_b32 %0, %3\n\ts_waitcnt lgkmcnt(0)\n\t"
               "v_readfirstlane_b32 %1, %0\n\ts_cmp_ge_i32 %1, %2\n\ts_cbranch_scc0 0b\n\ts_branch 1b\n"
               ".subsection 0"
               : "=&v"(v), "+s"(t) : "s"(need), "v"(addr) : "memory", "scc");
}
#define LSLP 0
DI void wait_all_gt(int& v, int p, unsigned addr) {
  asm volatile("v_cmp_lt_i32 vcc, %1, %0\n\ts_cmp_eq_u64 vcc, exec\n\ts_cbranch_scc0 0f\n1:\n"
               ".subsection 1\n"
               "0:\n\ts_sleep %3\n\tds_read_b32 %0, %2\n\ts_waitcnt lgkmcnt(0)\n\t"
               "v_cmp_lt_i32 vcc, %1, %0\n\ts_cmp_eq_u64 vcc, exec\n\ts_cbranch_scc0 0b\n\ts_branch 1b\n"
               ".subsection 0"
               : "+v"(v) : "s"(p), "v"(addr), "n"(LSLP) : "memory", "scc", "vcc");
}
DI void lds_inc(unsigned addr, unsigned one) {
  asm volatile("s_mov_b64 exec, 1\n\tds_add_u32 %0, %1\n\ts_mov_b64 exec, -1" :: "v"(addr), "v"(one) : "memory");
}
DI void glds16v(const char* p, unsigned lds_base) {
  asm volatile("s_mov_b32 m0, %1\n\ts_nop 0\n\tglobal_load_lds_dwordx4 %0, off" :: "v"(p), "s"(lds_base) : "memory", "m0");
}
DI void gtouch(const char* p, unsigned lds_base) {
  asm volatile("s_mov_b32 m0, %1\n\ts_nop 0\n\tglobal_load_lds_dword %0, off" :: "v"(p), "s"(lds_base) : "memory", "m0");
}
template <int N> DI void wait_vm() { asm volatile("s_waitcnt vmcnt(%0)" ::"n"(N) : "memory"); }
DI void wait_lgkm() { asm volatile("s_waitcnt lgkmcnt(0)" ::: "memory"); }
constexpr float PEPS = 1e-6f, LOG2E = 1.4426950408889634f;
DI f32x4 phi4(const f32x4& a) {
  int eps = __float_as_int(PEPS);
  asm("" : "+s"(eps));
  const f32x4 c1 = {LOG2E, LOG2E, LOG2E, LOG2E}, c0 = c1 * -PEPS;
  f32x4 e = __builtin_elementwise_fma(a, c1, c0), r;
#pragma unroll
  for (int j = 0; j < 4; ++j) {
    e[j] = __builtin_amdgcn_fmed3f(__builtin_amdgcn_exp2f(e[j]), 0.f, 1.f);
    const int ai = __float_as_int(a[j]);
    r[j] = __int_as_float(ai > eps ? ai : eps);
  }
  return r + e;
}
template <int CTRL> DI unsigned dpp_u(unsigned v) { return (unsigned)__builtin_amdgcn_update_dpp(0, (int)v, CTRL, 0xf, 0xf, true); }
DI unsigned umax(unsigned a, unsigned b) { return a > b ? a : b; }
DI unsigned umin(unsigned a, unsigned b) { return a < b ? a : b; }
DI float dot2(unsigned a, unsigned b, float c) {
  return __builtin_amdgcn_fdot2_f32_bf16(__builtin_bit_cast(bf16x2, a), __builtin_bit_cast(bf16x2, b), c, false);
}
typedef __amdgpu_buffer_rsrc_t rsrc_t;
DI rsrc_t make_rsrc(const void* p) { return __builtin_amdgcn_make_buffer_rsrc((void*)p, 0, 0x7fffffff, 0x00020000); }
#define K1AUX 16
typedef __attribute__((address_space(1))) unsigned gu32;
#define RLX_AGENT __ATOMIC_RELAXED, __HIP_MEMORY_SCOPE_AGENT
template <int AUX> DI bf16x8 ldf(rsrc_t r, unsigned off, int f) {
  return __builtin_bit_cast(bf16x8, __builtin_amdgcn_raw_buffer_load_b128(r, off + (f & 3) * 1024, (f >> 2) * 4096, AUX));
}
template <int AUX> DI bf16x8 ld16(rsrc_t r, unsigned off, unsigned soff) {
  return __builtin_bit_cast(bf16x8, __builtin_amdgcn_raw_buffer_load_b128(r, off, soff, AUX));
}
template <int AUX, class T> DI void st16(rsrc_t r, unsigned off, unsigned soff, T v) {
  __builtin_amdgcn_raw_buffer_store_b128(__builtin_bit_cast(u32x4, v), r, off, soff, AUX);
}
#define SPIN_(x) asm volatile("" : "+s"(x))

#define NOSTM 0u
#define LPRIO 2
#define INFL 5
#define CPFW 3
#define KROT 5
#define CLATE 1
#define XF 0
#define ADDR_P 14
#define W18 12
#define W19 6
#define XR 101
#define LASM 1
#define LOFF 0x7633
#define EBAR 0
#define MK0 3
#define MKE 1
#define FLS 0
constexpr int K1_WP = 1056;
constexpr int K1_WS = 16 * K1_WP;
constexpr int K1_XO = 5 * K1_WS;
constexpr int K1_XS = 16384;
constexpr int K1_FL = K1_XO + 4 * K1_XS;
constexpr int K1_PG = K1_FL + 32, K1_PS = 272;
constexpr int K1_LDS = K1_PG + 8 * K1_PS;

constexpr int k1_toff(int mode, int t) {
  return mode ? 32 * t : (t == 0 ? 0 : t == 1 ? 48 : t == 2 ? 160 : t == 3 ? 208 : t == 4 ? 32 : t == 5 ? 320 : 352);
}
constexpr bool k1_tr(int mode, int t) { return mode == 2 ? t >= 4 : mode == 0 && t < 5; }

DI void touch(const char* p) { asm volatile("global_load_dword v255, %0, off" :: "v"(p) : "memory", "v255"); }
DI void touch(const char* p, unsigned o) { asm volatile("global_load_dword v255, %0, %1" :: "v"(o), "s"(p) : "memory", "v255"); }
template <int OFF> DI void k1_post(rsrc_t tb, unsigned so) {
  asm volatile("buffer_load_dword off, %0, %1 offset:%2 lds" :: "s"(tb), "s"(so), "n"(OFF) : "memory");
}
template <int I> struct Idx { constexpr operator int() const { return I; } };
template <int N, int I = 0, class F> DI void static_for(F& f) {
  if constexpr (I < N) { f(Idx<I>{}); static_for<N, I + 1>(f); }
}
struct K1Adr { unsigned wp, wm, xe, xo, wso, xso, cnt; };
template <int MODE, int ODD>
DI void k1_step(f32x4 (&acc)[8][4], const bf16x8 (&wc)[8], const bf16x8 (&xc)[4], bf16x8 (&wn)[8], bf16x8 (&xn)[4],
                K1Adr& a, unsigned wb, unsigned wmb, unsigned xeb, unsigned xob, unsigned flp, rsrc_t tb, rsrc_t mk) {
  constexpr int NT = MODE ? 8 : 7, NR = 4 + 2 * NT, E = 4 * NT;
  constexpr int S_T0 = MODE ? NR + 1 : 2, S_WSO = MODE ? NR + 2 : NR + 1, S_WP = MODE ? NR + 3 : NR + 2, S_XS = MODE ? NR + 4 : 5,
                S_XE = MODE ? NR + 5 : NR + 3, S_XO = MODE ? NR + 6 : NR + 4;
  s16x4 lo;
  int fl;
  unsigned t0;
#pragma unroll
  for (int t = 0; t < NT; ++t)
#pragma unroll
    for (int m = 0; m < 4; ++m) {
      asm volatile("v_mfma_f32_16x16x32_bf16 %0, %1, %2, %0" : "+v"(acc[t][m]) : "v"(k1_tr(MODE, t) ? xc[m] : wc[t]), "v"(k1_tr(MODE, t) ? wc[t] : xc[m]));
      SBAR();
      const int s = t * 4 + m;
      if (s < 4) xn[s] = lds_r128(LP((s & 1) ? a.xo : a.xe) + (s >> 1) * 4096);
      else if (s < NR) {
        const int i = (s - 4) >> 1;
        const lds_ptr q = LP(MODE == 0 && i == 4 ? a.wm : a.wp) + k1_toff(MODE, i);
        if (!(s & 1)) lo = lds_tr(q);
        else wn[i] = cat8(lo, lds_tr(q + 512));
      }
      if (s == NR + FLS) fl = lds_ld(LP(flp));
      if (s == S_T0) { SPIN_(a.wso); t0 = a.wso + K1_WS; SPIN_(t0); }
      if (s == S_WSO) { a.wso = a.wso == 4 * K1_WS ? 0 : t0; SPIN_(a.wso); }
      if (s == S_WP) {
        a.wp = wb + a.wso; PIN(a.wp);
        if (MODE == 0) { a.wm = wmb + a.wso; PIN(a.wm); }
      }
      if (s == S_XS) {
        if (ODD) { SPIN_(a.cnt); a.cnt += 8; SPIN_(a.cnt); }
        else { SPIN_(a.xso); a.xso += K1_XS; SPIN_(a.xso); }
      }
      if (s == S_XS + !MODE && !ODD) { a.xso &= 4 * K1_XS - 1; SPIN_(a.xso); }
      if (s == S_XE) asm volatile("v_add_u32 %0, %1, %2" : "=v"(a.xe) : "s"(a.xso), "v"(xeb));
      if (s == S_XO) asm volatile("v_add_u32 %0, %1, %2" : "=v"(a.xo) : "s"(a.xso), "v"(xob));
      if (s == E - 5) {
        if (ODD) asm volatile("v_cmp_gt_u32 vcc, %1, %0" : "+v"(fl) : "s"(a.cnt) : "vcc");
        else asm volatile("v_cmp_ge_u32 vcc, %1, %0" : "+v"(fl) : "s"(a.cnt) : "vcc");
      }
      if (s == E - 3) asm volatile("s_cmp_gt_u32 %0, %1" :: "s"(a.cnt), "n"(ODD ? 64 : 56) : "scc");
      constexpr int NM = MODE ? 5 - MK0 : MK0, M0 = ODD ? (MKE < NM ? MKE : NM) : 0, M1 = ODD || MKE > NM ? NM : MKE;
      if (s == E - 2)
        asm volatile("s_cbranch_scc1 0f\n\tbuffer_load_dword off, %0, %1 offset:%2 lds\n1:\n.subsection 1\n0:\n\t"
                     ".set .Lgm_o, %4\n\t.rept %5\n\tbuffer_store_dwordx4 v[248:251], v252, %3, 0 offen offset:.Lgm_o sc1\n\t"
                     ".set .Lgm_o, .Lgm_o + 1024\n\t.endr\n\ts_branch 1b\n.subsection 0"
                     :: "s"(tb), "s"(a.cnt), "n"(ODD ? 0 : 4), "s"(mk), "n"(M0 * 1024), "n"(M1 - M0) : "memory");
      if (s == E - 4) {
#define K1_WAIT(CMP)                                                                                     \
  asm volatile("s_cbranch_vccnz 0f\n1:\n.subsection 1\n"                                                 \
               "0:\n\tds_read_b32 %0, %2\n\ts_waitcnt lgkmcnt(0)\n\t" CMP " vcc, %1, %0\n\t"            \
               "s_cbranch_vccnz 0b\n\ts_branch 1b\n.subsection 0"                                        \
               : "+v"(fl) : "s"(a.cnt), "v"(flp) : "memory", "vcc")
        if (ODD) K1_WAIT("v_cmp_gt_u32"); else K1_WAIT("v_cmp_ge_u32");
      }
      SBAR();
    }
}

DI bf16x8 pack8(const f32x4& u, const f32x4& v) {
  return __builtin_bit_cast(bf16x8, u32x4{cvtpk(u[0], u[1]), cvtpk(u[2], u[3]), cvtpk(v[0], v[1]), cvtpk(v[2], v[3])});
}
#define KEEP2(d) asm volatile("" :: "v"(d[0]), "v"(d[1]))
#define JOINT 1
#define ORD2 0x76325140
constexpr int k1_ord(int mode, int i) { return ((mode == 2 ? ORD2 : mode ? 0x76543210 : 0x6532410) >> (4 * i)) & 15; }
constexpr int k1_grp(int g) { return g >= 6 ? g / 3 | (g % 3) << 4 : (int)(((JOINT == 1 ? 0x212011011000ull : JOINT == 2 ? 0x211120011000ull : 0x211101201000ull) >> (8 * g)) & 255); }
constexpr int k1_end(int mode, int i) { return i ? 12 * i + (!mode && i == 6 ? 25 : 11) : (JOINT == 1 ? 19 : JOINT == 2 ? 15 : 11); }
constexpr int k1_kind(int mode, int var, int t) {
  return mode == 2 ? (t < 4 ? 3 : 0) : mode == 1 ? 1 + var : (t < 2 ? 4 : t < 4 ? 6 : t == 4 ? 5 : 1);
}
constexpr int k1_units(int kind, int t) { return kind == 0 ? 6 : kind == 1 ? 8 : kind == 2 ? ((t & 3) == 3 ? 7 : 4) : kind == 3 ? 15 : kind == 4 ? 14 : kind == 5 ? 20 : 4; }
constexpr int k1_slot(int mode, int var, int i, int j) {
  int s = 0;
  for (int q = 0; q <= i; ++q) {
    if (s <= k1_end(mode, q)) s = k1_end(mode, q) + 1;
    s += q < i ? k1_units(k1_kind(mode, var, k1_ord(mode, q)), k1_ord(mode, q)) : j;
  }
  return s;
}
#define K1_WAITN(N)                                                                                      \
  asm volatile("s_cbranch_vccnz 0f\n1:\n.subsection 1\n"                                                 \
               "0:\n\tds_read_b32 %0, %2\n\ts_waitcnt lgkmcnt(0)\n\tv_cmp_gt_u32 vcc, %1, %0\n\t"        \
               "s_cbranch_vccnz 0b\n\ts_branch 1b\n.subsection 0"                                        \
               : "+v"(fl) : "n"(N), "v"(flp) : "memory", "vcc")
struct K1Warm { const char* pb; unsigned sov; rsrc_t tb; };
template <int MODE, int V>
DI void k1_compute(char* smem, char* ws, int rt, int idx, int w, int lane, const K1Warm g) {
  constexpr int NT = MODE ? 8 : 7;
  const unsigned lds0 = (unsigned)(size_t)smem;
  const int g4 = lane >> 4, c16 = lane & 15, th = w & 1, ch = w >> 1, hn = 2 * idx + ch;
  unsigned wb = lds0 + (4 * g4 + (c16 >> 2)) * K1_WP + ch * (MODE ? 256 : 80) + (c16 & 3) * 8;
  unsigned wmb = wb + (c16 & 2) * 72;
  const int re = MODE == 2 ? 8 * (c16 >> 2) + (c16 & 3) : c16, ro = re + (MODE == 2 ? 4 : 16);
  unsigned xe0 = lds0 + K1_XO + (th * 64 + re) * 128 + ((g4 ^ ((re >> 1) & 7) ^ ((re >> 3) & 2)) << 4);
  unsigned xo0 = lds0 + K1_XO + (th * 64 + ro) * 128 + ((g4 ^ ((ro >> 1) & 7) ^ ((ro >> 3) & 2)) << 4);
  unsigned xe1 = xe0 ^ 64, xo1 = xo0 ^ 64;
  unsigned flp = lds0 + K1_PG + (4 + (lane & 3)) * K1_PS + 4;
  PIN(flp); PIN(wb); PIN(xe0); PIN(xo0); PIN(xe1); PIN(xo1);
  if (MODE == 0) PIN(wmb);
  rsrc_t rs = make_rsrc(ws);
  SPIN_(rs);
  asm volatile("v_mov_b32 v248, -1\n\tv_mov_b32 v249, -1\n\tv_mov_b32 v250, -1\n\tv_mov_b32 v251, -1\n\tv_mov_b32 v252, %0"
                 :: "v"((unsigned)WS_O + (rt * 8 + idx) * 40960 + 20480 + w * 5120 + (MODE ? MK0 * 1024 : 0) + lane * 16) : "v248", "v249", "v250", "v251", "v252");
  f32x4 acc[8][4];
#pragma unroll
  for (int t = 0; t < 8; ++t) {
    const float a0 = MODE == 0 ? (t == 4 ? (c16 < 8 ? PEPS : 0.f) : t < 2 ? PEPS : 0.f) : (MODE == 2 && t < 4 ? PEPS : 0.f);
#pragma unroll
    for (int m = 0; m < 4; ++m) { acc[t][m] = f32x4{a0, a0, a0, a0}; if (t < NT) PIN(acc[t][m]); }
  }
  bf16x8 wa[8], wc[8], xa[4], xc[4];
  int fl;
  K1Adr a = {wb + K1_WS, wmb + K1_WS, xe1, xo1, K1_WS, 0, 8};
  PIN(a.wp); PIN(a.xe); PIN(a.xo);
  if (MODE == 0) PIN(a.wm);
  asm volatile("s_mov_b32 m0, %0" :: "s"(lds0 + K1_PG + w * K1_PS) : "m0");
  SBAR();
  asm volatile("0:\n\tds_read_b32 %0, %1\n\ts_waitcnt lgkmcnt(0)\n\tv_cmp_eq_u32 vcc, 0, %0\n\ts_cbranch_vccnz 0b"
               : "=&v"(fl) : "v"(flp) : "memory", "vcc");

#pragma unroll
  for (int s = 0; s < 4; ++s) xa[s] = lds_r128(LP((s & 1) ? xo0 : xe0) + (s >> 1) * 4096);
#pragma unroll
  for (int i = 0; i < NT; ++i) {
    const lds_ptr q = LP(MODE == 0 && i == 4 ? wmb : wb) + k1_toff(MODE, i);
    wa[i] = cat8(lds_tr(q), lds_tr(q + 512));
  }
  if (w >= 2) touch(g.pb);
  fl = lds_ld(LP(flp));
  SPIN_(a.cnt);
  asm volatile("v_cmp_gt_u32 vcc, %1, %0" : "+v"(fl) : "s"(a.cnt) : "vcc");
  k1_post<0>(g.tb, a.cnt);
  K1_WAIT("v_cmp_gt_u32");
  SBAR();
  asm volatile(".p2align 5\n\t.rept %0\n\ts_nop 0\n\t.endr" :: "n"((LOFF >> (4 * (MODE + 2 * V))) & 15));
  SBAR();
#pragma unroll 1
  do {
    k1_step<MODE, 0>(acc, wa, xa, wc, xc, a, wb, wmb, xe0, xo0, flp, g.tb, rs);
    k1_step<MODE, 1>(acc, wc, xc, wa, xa, a, wb, wmb, xe1, xo1, flp, g.tb, rs);
  } while (a.cnt != 72);
  SBAR();
  const unsigned l16 = lane * 16;
  bf16x8 xt[3][4], wt[8][3];
  s16x4 lo;
  auto mul = [&](int t, const bf16x8& wf, const bf16x8& xf, int m) {
    asm volatile("v_mfma_f32_16x16x32_bf16 %0, %1, %2, %0" : "+v"(acc[t][m]) : "v"(k1_tr(MODE, t) ? xf : wf), "v"(k1_tr(MODE, t) ? wf : xf));
  };
  auto xrd = [&](int q, int s) { xt[q][s] = lds_r128(LP((s & 1) ? (q & 1 ? xo0 : xo1) : (q & 1 ? xe0 : xe1)) + (s >> 1) * 4096 + (q ? K1_XS : 0)); };
  auto wrd = [&](int i, int q, int h) {
    const int t = k1_ord(MODE, i);
    const lds_ptr p = LP(MODE == 0 && t == 4 ? a.wm : a.wp) + k1_toff(MODE, t) + q * K1_WS;
    if (!h) lo = lds_tr(p);
    else wt[i][q] = cat8(lo, lds_tr(p + 512));
  };
#pragma unroll
  for (int s = 0; s < 4 * NT; ++s) {
    mul(s >> 2, wa[s >> 2], xa[s & 3], s & 3);
    if (s < 4) xrd(0, s);
    else if (s < 6) wrd(0, 0, s & 1);
    else if (s < 8) wrd(1, 0, s & 1);
    if (s == W18 - 6) fl = lds_ld(LP(flp));
    if (s == W18 - 1) asm volatile("v_cmp_gt_u32 vcc, 76, %0" : "+v"(fl) :: "vcc");
    if (s == W18) K1_WAITN(76);
    if (s > W18 && s <= W18 + 4) xrd(1, s - W18 - 1);
    else if (s > W18 + 4 && s <= W18 + 6) wrd(0, 1, s - W18 - 5);
    else if (s > W18 + 6 && s <= W18 + 8) wrd(1, 1, s - W18 - 7);
    else if (s > W18 + 8 && s <= W18 + 12) xrd(2, s - W18 - 9);
    if (s == 4 * NT + W19 - 6) fl = lds_ld(LP(flp));
    if (s == 4 * NT + W19 - 1) asm volatile("v_cmp_gt_u32 vcc, 80, %0" : "+v"(fl) :: "vcc");
    SBAR();
  }
  {
    constexpr int NS = MODE ? 96 : 98;
    unsigned so[8], mx[4], pk[4];
    u32x4 d[8][2], ka[3][2], vv[2][2], mb[2];
    float e[2][4], n0, n1, c0 = -PEPS * LOG2E, qs = QSCALE;
    f32x2 c1 = {LOG2E, LOG2E}, c2 = {c0, c0};
    f32x4 r[7];
    int eps = __float_as_int(PEPS), f;
    unsigned nst = MODE == 2 ? (g4 >> 1) * 512 + ((g4 & 1) ? ro : re) * 16 : l16;
    SPIN_(eps); SPIN_(qs); SPIN_(c1); PIN(c2); PIN(nst);
    c0 = c2[0];
    f32x4* xch = (f32x4*)(smem + K1_XO + 2 * K1_XS + ch * 4096) + lane;
    const unsigned pf = lds0 + K1_FL + ch * 8;
    auto unit = [&](auto I_, auto J_, auto S_) {
      constexpr int t = k1_ord(MODE, I_), kind = k1_kind(MODE, V, t), J = J_;
      f32x4* y = acc[t];
      auto cv2 = [&](int k) {
        unsigned lo = cvtpk(y[k][0], y[k][1]), hi = cvtpk(y[k][2], y[k][3]);
        PIN(lo); PIN(hi);
        if (kind >= 1 && kind <= 3) { pk[2 * (k & 1)] = lo; pk[2 * (k & 1) + 1] = hi; }
        else {
          u32x4& o = kind == 0 ? d[t][k >> 1] : kind == 6 ? vv[t & 1][k >> 1] : kind == 5 && J < 6 ? mb[k >> 1] : ka[kind == 5 ? 2 : t & 1][k >> 1];
          o[2 * (k & 1)] = lo; o[2 * (k & 1) + 1] = hi;
        }
      };
      auto put = [&](int mp) {
        auto s0 = __builtin_amdgcn_permlane16_swap(pk[0], pk[2], false, false);
        auto s1 = __builtin_amdgcn_permlane16_swap(pk[1], pk[3], false, false);
        d[t][mp] = u32x4{s0[0], s1[0], s0[1], s1[1]};
        st16<K1AUX>(rs, (mp ? nst + (kind == 2 ? REC : QREC) : nst) | NOSTM, so[MODE ? t : t - 5], d[t][mp]);
      };
      if constexpr (kind == 0) {
        if (J % 3 < 2) cv2(J / 3 * 2 + J % 3);
        else st16<K1AUX>(rs, (J > 3 ? l16 + REC : l16) | NOSTM, so[t], d[t][J / 3]);
      } else if constexpr (kind == 6) {
        cv2(J);
      } else if constexpr (kind == 1) {
        constexpr int mp = J >> 2, j = J & 3;
        if (j < 2) {
          if (S_ >= NS) y[2 * mp + j] *= QSCALE;
          else {
#pragma unroll
            for (int i = 0; i < 4; ++i) { float v = y[2 * mp + j][i] * qs; PIN(v); y[2 * mp + j][i] = v; }
          }
        } else if (j == 2) { cv2(2 * mp); cv2(2 * mp + 1); }
        else put(mp);
      } else if constexpr (kind == 2) {
        const bool b0 = lane & 1, b1 = lane & 2;
        if (J == 0 || J == 2) {
          cv2(J); cv2(J + 1);
          SBAR();
          asm volatile("v_dot2_f32_bf16 %0, %2, %2, 0\n\tv_dot2_f32_bf16 %1, %4, %4, 0\n\tv_dot2_f32_bf16 %0, %3, %3, %0\n\tv_dot2_f32_bf16 %1, %5, %5, %1"
                       : "=&v"(n0), "=&v"(n1) : "v"(pk[0]), "v"(pk[1]), "v"(pk[2]), "v"(pk[3]));
        } else if (J < 4) {
          put(J >> 1);
          SBAR();
          mx[t & 3] = umax(J == 3 ? umax(mx[t & 3], __float_as_uint(n0)) : __float_as_uint(n0), __float_as_uint(n1));
          PIN(mx[t & 3]);
        } else if (J == 4) {
          mx[0] = umax(b0 ? mx[1] : mx[0], dpp_u<0xB1>(b0 ? mx[0] : mx[1]));
          mx[2] = umax(b0 ? mx[3] : mx[2], dpp_u<0xB1>(b0 ? mx[2] : mx[3]));
        } else if (J == 5) {
          mx[0] = umax(b1 ? mx[2] : mx[0], dpp_u<0x4E>(b1 ? mx[0] : mx[2]));
          mx[0] = umax(mx[0], dpp_u<0x124>(mx[0]));
        } else {
          mx[0] = umax(mx[0], dpp_u<0x128>(mx[0]));
          __builtin_amdgcn_raw_buffer_store_b32(mx[0], rs, c16 < 4 ? (4 * c16 + g4) * 128 + (t >> 2) * 2048 : 0x80000000u,
                                                (unsigned)WS_KMAX + (hn - 10) * 4096 + (rt * 2 + th) * 4, 0);
        }
      } else if constexpr (kind == 5 && J < 6) {
        if (J % 3 < 2) cv2(J / 3 * 2 + J % 3);
        else {
          if (c16 < 8) mb[J / 3] = u32x4{0x3f803f80u, 0x3f803f80u, 0x3f803f80u, 0x3f803f80u};
          PIN(mb[J / 3]);
        }
      } else if constexpr (kind == 3 && J == 10) {
        put(0);
      } else {
        constexpr int jj = kind == 5 ? J - 6 : kind == 3 && J > 10 ? J - 1 : J, k = (jj + 2) / 3 - 1, p = (jj + 2) % 3;
        auto ex = [&](int i) { e[k & 1][i] = __builtin_amdgcn_fmed3f(__builtin_amdgcn_exp2f(e[k & 1][i]), 0.f, 1.f); PIN(e[k & 1][i]); };
        auto mxe = [&](int i) {
          int v = __float_as_int(y[k][i]);
          v = v > eps ? v : eps;
          if (i < 2) PIN(v);
          y[k][i] = __int_as_float(v);
        };
        if constexpr (jj == 0) {
#pragma unroll
          for (int i = 0; i < 4; ++i) { e[0][i] = __builtin_fmaf(y[0][i], LOG2E, c0); PIN(e[0][i]); }
        } else if constexpr (jj == 13) {
          cv2(3);
          if (kind == 3) put(1);
        } else if constexpr (p == 0) {
          ex(0); ex(1);
          SBAR();
          if constexpr (k < 3) {
#pragma unroll
            for (int i = 0; i < 4; i += 2) {
              f32x2 v;
              asm volatile("v_pk_fma_f32 %0, %1, %2, %3" : "=v"(v) : "v"(f32x2{y[k + 1][i], y[k + 1][i + 1]}), "s"(c1), "v"(c2));
              e[~k & 1][i] = v[0]; e[~k & 1][i + 1] = v[1];
            }
          }
        } else if constexpr (p == 1) {
          ex(2); ex(3); mxe(0); mxe(1);
        } else {
          mxe(2); mxe(3);
          if constexpr (k > 0) cv2(k - 1);
          SBAR();
          f32x2 lo = {y[k][0], y[k][1]}, hi = {y[k][2], y[k][3]};
          asm volatile("v_pk_add_f32 %0, %0, %2\n\tv_pk_add_f32 %1, %1, %3" : "+v"(lo), "+v"(hi)
                       : "v"(f32x2{e[k & 1][0], e[k & 1][1]}), "v"(f32x2{e[k & 1][2], e[k & 1][3]}));
          y[k] = f32x4{lo[0], lo[1], hi[0], hi[1]};
        }
      }
    };
    auto slot = [&](auto S_) {
      constexpr int s = S_;
      if constexpr (MODE == 0 && s >= 72 && s < 86) {
        constexpr int c = (0x52524063140631ull >> (4 * (s - 72))) & 15, q = (0x33e0 >> (s - 72)) & 1;
        if (!q) r[c] = f32x4{0, 0, 0, 0};
        r[c] = MFMA16(__builtin_bit_cast(bf16x8, ka[c < 6 ? c >> 1 : 2][q]),
                      __builtin_bit_cast(bf16x8, c == 0 || c == 4 ? vv[0][q] : c == 2 || c == 5 ? vv[1][q] : mb[q]), r[c]);
        SBAR();
      } else if constexpr (s < NS) {
        constexpr int z = MODE == 0 && s >= 72 ? s - 14 : s, i = k1_grp(z >> 2) & 15, q = k1_grp(z >> 2) >> 4;
        mul(k1_ord(MODE, i), wt[i][q], xt[q][z & 3], z & 3);
        SBAR();
        if (s == W19 - 6) fl = lds_ld(LP(flp));
        if (s == W19 - 1) asm volatile("v_cmp_gt_u32 vcc, 80, %0" : "+v"(fl) :: "vcc");
        if (s == W19) K1_WAITN(80);
        if (s > W19 && s <= W19 + 4) wrd((s - W19 - 1) >> 1, 2, (s - W19 - 1) & 1);
        if (s >= 12 && s % 12 < 6 && s / 12 + 1 < NT) wrd(s / 12 + 1, (s % 12) >> 1, s & 1);
        if (s >= 23 && s % 12 == 11 && s / 12 + 1 < NT) __builtin_amdgcn_s_waitcnt(0xc07f);
        if (s == NS - 1) asm volatile("s_nop 3");
        if constexpr (s < (MODE ? 8 : 3)) { so[s] = __builtin_amdgcn_readlane(g.sov, s); SPIN_(so[s]); }
      }
      auto tile = [&](auto I_) {
        constexpr int t = k1_ord(MODE, I_), s0 = k1_slot(MODE, V, I_, 0);
        if constexpr (s >= s0 && s < s0 + k1_units(k1_kind(MODE, V, t), t)) unit(I_, Idx<s - s0>{}, S_);
      };
      static_for<NT>(tile);
      if constexpr (MODE == 0) {
        if (s == 86) { if (c16 == 1 || c16 >= 12) r[1] = r[3]; PIN(r[1]); }
        if (s == 87) { if (g4) r[4] = r[5]; PIN(r[4]); }
        if (s == 88 || s == 89) {
#pragma unroll
          for (int i = 2 * (s & 1); i < 2 * (s & 1) + 2; ++i)
            r[6][i] = __uint_as_float(__builtin_amdgcn_permlane32_swap(__float_as_uint(r[6][i]), __float_as_uint(r[4][i]), false, false)[0]);
          PIN(r[6]);
        }
        if (s == 90) {
          if (th) { xch[128] = r[0]; xch[192] = r[1]; r[0] = r[2]; r[1] = r[6]; }
          else { xch[0] = r[2]; xch[64] = r[6]; }
          lds_inc(pf + th * 4, 1);
        }
        if (s == XR - 3) f = lds_ld(LP(pf + 4 - th * 4));
        if (s == XR) {
          wait_ge(f, 1, pf + 4 - th * 4);
          xch += 128 - th * 128;
          r[2] = xch[0]; r[3] = xch[64];
        }
        if (s == 105) {
          r[0] += r[2]; r[1] += r[3];
          st16<K1AUX>(rs, l16, so[2], r[0]);
          st16<K1AUX>(rs, l16 + 2048, so[2], r[1]);
        }
      }
      SBAR();
    };
    constexpr int TL = k1_ord(MODE, NT - 1);
    static_for<k1_slot(MODE, V, NT - 1, k1_units(k1_kind(MODE, V, TL), TL))>(slot);
#pragma unroll
    for (int t = MODE ? 0 : 5; t < NT; ++t) KEEP2(d[t]);
  }
}

__global__ __launch_bounds__(512, 2) __attribute__((amdgpu_num_vgpr(248))) void k1_qkv(const char* __restrict__ x, const char* __restrict__ wa,
                                                 const char* __restrict__ wb, const char* __restrict__ woa,
                                                 const char* __restrict__ wob, const char* __restrict__ gw,
                                                 char* __restrict__ ws) {
  extern __shared__ __attribute__((aligned(16))) char smem[];
  const int tid = threadIdx.x, lane = tid & 63, w = rfl(tid >> 6);
  const int b = blockIdx.x, xcd = b & 7, bi = b >> 3;
  const int rt = (xcd & 1) * 8 + (bi & 7), ct = (xcd >> 1) * 4 + (bi >> 3);
  const int type = ct & 1, idx = ct >> 1;
  const unsigned lds0 = (unsigned)(size_t)smem;
  const char* pc; unsigned len;
  asm volatile("s_getpc_b64 %0\n1:\n\ts_mov_b32 %1, .Lgm_text_end - 1b - 96\n.subsection 2\n\t.p2align 7\n"
               "\t.long 0, 0, 4, 8, 12, 16, 20, 24, 28, 32, 36, 40, 44, 48, 52, 56, 60, 64, 68, 72, 76, 400, 400, 400\n"
               ".Lgm_text_end:\n.subsection 0" : "=s"(pc), "=s"(len));
  if (w == CPFW) {
    const unsigned o = (bi & 7) * 8192 + lane * 128;
    touch(pc, o < len ? o : len);
  }
  lds_st(LP(lds0 + K1_PG - 32 * !w + w * K1_PS) + lane * 4, 0);
  if (EBAR) asm volatile("s_cmp_gt_u32 %0, 3\n\ts_cbranch_scc1 0f\n\ts_waitcnt lgkmcnt(0)\n\ts_barrier\n0:" :: "s"(w) : "memory", "scc");
  rsrc_t tb = make_rsrc(pc + len);
  SPIN_(tb);
  SBAR();
  if (w < 4) {
    const int sl = bi >> 2;
    unsigned to = w < 2 ? bi * 589824 + w * 294912
                        : (w == 2 ? ((bi & (sl < 6 ? 7 : 3)) + 1) * (sl < 4 ? 307200 : sl < 6 ? 327680 : 204800) - 4 : 0);
    SPIN_(to);
    SBAR();
    wait_lgkm();
    {
      const int q = bi & 3;
      const char* tp = w < 2 ? ws : w == 2 ? (sl < 2 ? wa : sl < 4 ? wb : sl < 6 ? x : sl == 6 ? woa : wob)
                                           : (q == 0 ? gw : q == 1 ? woa : q == 2 ? wob : pc);
      asm volatile("s_load_dword s101, %0, %1" :: "s"(tp), "s"(to) : "memory", "s101");
      if (!EBAR) asm volatile("s_barrier" ::: "memory");
    }
    SBAR();
    const int hn = 2 * idx + (w >> 1);
    const unsigned kt = rt * 4 + (w & 1) * 2;
    const unsigned P = !type ? 40 + 2 * hn + lane : hn < 10 ? 4 * hn + lane + (lane & 4) * 29 : (hn < 15 ? 8 * hn : 72) + lane;
    const unsigned G = __umul24(P, 205) >> 10;
    unsigned sov = (P - G * 5) * 1024 + (G < 16 ? (unsigned)WS_QB + __umul24((G ^ 8) * 64 + kt, QREC)
                                                : (unsigned)WS_KV + __umul24(((G & 7) * 64 + kt) * 2 + ((G >> 3) & 1), QREC));
    if (!type && lane == 2) sov = (unsigned)WS_KVP + (hn * 16 + rt) * 4096 + (w & 1) * 1024;
    const char* pb = gw + (lane < 10 ? lane : 9) * 128;
    if (w == 2) {
      const int u = b + (lane >> 5) * 256 < 400 ? b + (lane >> 5) * 256 : b;
      const int ks = u / 10, cc0 = 4 * ks + ((lane >> 3) & 3), br = cc0 / 80, cc = cc0 - br * 80, jj = cc / 5, c5 = cc - jj * 5;
      const int q0 = c5 < 4 ? (2 * jj + (c5 >> 1)) * 20 + 8 * (c5 & 1) : 2 * jj * 20 + 16;
      const int q = q0 + (lane & 3) + ((lane & 4) ? (c5 < 4 ? 4 : 20) : 0);
      pb = (br ? wob : woa) + (q * (DM * 2) + (u - ks * 10) * 128);
    }
    PIN(sov); PIN(pb);
    const K1Warm warm = {pb, sov, tb};
#define K1_FIN __builtin_amdgcn_endpgm();
    if (!type) { k1_compute<0, 0>(smem, ws, rt, idx, w, lane, warm); K1_FIN }
    if (hn < 10) { k1_compute<2, 0>(smem, ws, rt, idx, w, lane, warm); K1_FIN }
    if (hn < 15) { k1_compute<1, 1>(smem, ws, rt, idx, w, lane, warm); K1_FIN }
    k1_compute<1, 0>(smem, ws, rt, idx, w, lane, warm);
    K1_FIN
  }
  {
    const int lw = w - 4;
    __builtin_amdgcn_s_setprio(LPRIO);
    const int r0 = 16 * lw + (lane >> 3), r1 = r0 + 8;
    unsigned xv0 = r0 * (DM * 2) + (((lane & 7) ^ ((r0 >> 1) & 7) ^ ((r0 >> 3) & 2)) << 4);
    unsigned xv1 = r1 * (DM * 2) + (((lane & 7) ^ ((r1 >> 1) & 7) ^ ((r1 >> 3) & 2)) << 4);
    unsigned xd = lds0 + K1_XO + lw * 2048, wd = lds0 + lw * 4 * K1_WP, xo = rt * 128 * (DM * 2);
    const unsigned e = lane & 31;
    unsigned d1, d2, d3, g0, selm, t0_;
    asm("s_cmp_lt_u32 %6, 5\n\ts_cselect_b32 %0, 2432, 0\n\ts_cselect_b32 %1, -2560, 0\n\ts_cselect_b32 %2, 8, 9\n\t"
        "s_cselect_b32 %3, 0, -1280\n\ts_cselect_b32 %4, 0xff00ff00, -1\n\ts_lshl_b32 %2, %6, %2\n\ts_add_u32 %3, %3, %2\n\t"
        "s_cmp_eq_u32 %6, 7\n\ts_cselect_b32 %1, -1536, %1\n\ts_mul_i32 %2, %6, 160\n\ts_addk_i32 %2, 1280\n\t"
        "s_lshl_b32 %5, %6, 5\n\ts_sub_u32 %5, -2720, %5\n\ts_cmp_lg_u32 %7, 0\n\ts_cselect_b32 %3, %3, %2\n\t"
        "s_cselect_b32 %2, %0, -16\n\ts_cselect_b32 %0, %0, 1120\n\ts_cselect_b32 %1, %1, %5\n\ts_cselect_b32 %4, %4, 0xfff00000"
        : "=&s"(d1), "=&s"(d2), "=&s"(d3), "=&s"(g0), "=&s"(selm), "=&s"(t0_) : "s"(idx), "s"(type) : "scc");
    unsigned wo = g0 + 16 * umin(e, 28 + 3 * type) + (8 * lw + 4 * (lane >> 5)) * (N3 * 2);
    wo += (e >= 10 - 2 * type ? d1 : 0) + (e >= 20 - 4 * type ? d2 : 0) + (e >= 25 - type ? d3 : 0);
    unsigned dnp = lds0 + K1_PG + (lane & 3) * K1_PS + 4, pg = lds0 + K1_PG + w * K1_PS;
    const int rr = bi & 7, cc_ = bi >> 3;
    int k0 = KROT == 1 ? (rr * 5 + cc_ * 10) >> 2 : KROT == 2 ? (rr * 5 + cc_ * 10 + 2) >> 2
           : KROT == 3 ? ((rr * 5) >> 2) + ((cc_ * 5) >> 1) : KROT == 4 ? (rr * 5 + cc_ * 10 + (xcd >> 1) * 5 / 2) >> 2
           : KROT == 5 ? (((rr + cc_) & 3) * 5) >> 1 : KROT == 6 ? ((rr + cc_) & 1) * 5 : KROT == 7 ? ((rr + cc_) % 3 * 10) / 3
           : KROT == 8 ? ((((rr + cc_) & 3) * 5) >> 1) + (xcd >> 1) : KROT == 9 ? ((rr + cc_) & 1) * 5 + (xcd >> 1) : 0;
    k0 = k0 < 10 ? k0 : k0 - 10;
    wo += k0 * 64 * N3 * 2; xo += k0 * 128;
    const int wrap = 19 - 2 * k0;
    PIN(xv0); PIN(xv1); PIN(wo); PIN(dnp);
    asm volatile("" : "+s"(xd), "+s"(wd), "+s"(xo), "+s"(pg));
    SBAR();
    wait_lgkm();
    const char* xs = x + xo;
    glds16(xv0, xs, xd); glds16(xv1, xs, xd + 1024);
    const char* wp = (__builtin_amdgcn_inverse_ballot_w64((unsigned long long)selm << 32 | selm) ? wb : wa) + wo;
    const char* wq[4];
#pragma unroll
    for (int i = 0; i < 4; ++i) wq[i] = wp + i * (N3 * 2);
    unsigned wso = 0, xso = 0;
    int xinc = 64 * DM * 2, dn = 0;
    auto xload = [&](int h) {
      xs += h == wrap ? xinc - DM * 2 : xinc; xinc = 128 - xinc;
      xso = (xso + 8192) & 65535;
      glds16(xv0, xs, xd + xso); glds16(xv1, xs, xd + xso + 1024);
    };
    auto issue = [&](int j) {
#pragma unroll
      for (int i = 0; i < 4; ++i) { glds16v(wq[i], wd + wso + i * K1_WP); }
      wso = wso == 4 * K1_WS ? 0 : wso + K1_WS;
      if (j) dn = lds_ld(LP(dnp));
      if (j < 19 && (!j || j > 2 * XF)) xload(j);
      asm volatile("s_mov_b32 m0, %2\n\ts_nop 0\n\tbuffer_load_dword off, %0, %1 lds" :: "s"(tb), "s"(4 * j + 8), "s"(pg) : "memory", "m0");
      if (XF && j && j <= XF) { xload(2 * j - 1); xload(2 * j); }
#pragma unroll
      for (int i = 0; i < 4; ++i) wq[i] += j == wrap ? -608 * N3 * 2 : 32 * N3 * 2;
    };
    issue(0);
    asm volatile("s_barrier" ::: "memory");
    SBAR();
    unsigned gxo, ggo, gso, uo[2][2], us[2];
    const char* ub[2];
    u32x2 gx[5], gg[5];
    unsigned ue[2][8];
    asm volatile("" : "=v"(gxo), "=v"(ggo), "=v"(gso), "=v"(uo[0][0]), "=v"(uo[0][1]), "=v"(uo[1][0]), "=v"(uo[1][1]),
                      "=s"(us[0]), "=s"(us[1]), "=s"(ub[0]), "=s"(ub[1]));
#pragma unroll
    for (int j = 0; j < 5; ++j) asm volatile("" : "=v"(gx[j]), "=v"(gg[j]));
#pragma unroll
    for (int j = 0; j < 8; ++j) asm volatile("" : "=v"(ue[0][j]), "=v"(ue[1][j]));
    auto side_loads = [&]() {
#pragma unroll
      for (int i = 0; i < 2; ++i)
        if (!i || b < 144) {
#pragma unroll
          for (int j = 0; j < 8; ++j)
            asm volatile("global_load_ushort %0, %1, %2 offset:%3" : "=&v"(ue[i][j]) : "v"(uo[i][j >> 2]), "s"(ub[i]), "n"((j & 3) * DM * 2) : "memory");
        }
#pragma unroll
      for (int j = 0; j < 5; ++j)
        asm volatile("global_load_dwordx2 %0, %2, %3 offset:%6\n\tglobal_load_dwordx2 %1, %4, %5 offset:%6"
                     : "=&v"(gx[j]), "=&v"(gg[j]) : "v"(gxo), "s"(x), "v"(ggo), "s"(gw), "n"(j * 256) : "memory");
    };
    SBAR();
#pragma unroll 1
    for (int p = -4; p < 15; ++p) {
      PIN(dn);
      if (INFL < 5) wait_vm<7 * INFL - 7>();
      wait_all_gt(dn, 4 * p, dnp);
      issue(p + 5);
      if (p == ADDR_P) {
        int bb = b, ln = lane;
        SPIN_(bb); PIN(ln);
        const int row = rt * 128 + ct * 8 + lw * 2 + (ln >> 5);
        ggo = (ln & 31) * 8; gxo = row * (DM * 2) + ggo; gso = (unsigned)WS_G + row * 4;
#pragma unroll
        for (int i = 0; i < 2; ++i) {
          const int u = bb + i * 256 < 400 ? bb + i * 256 : bb;
          const int ks = (u * 205) >> 11, nt = (u - ks * 10) * 4 + lw, br = ks >= 20;
          const int cc = 4 * ks - br * 80 + (ln >> 4), j = (cc * 205) >> 10, c5 = cc - j * 5;
          const int q0 = j * 40 + (c5 < 4 ? (c5 >> 1) * 20 + 8 * (c5 & 1) : 16);
          uo[i][0] = q0 * (DM * 2) + (16 * nt + (ln & 15)) * 2;
          uo[i][1] = uo[i][0] + (c5 < 4 ? 4 : 20) * (DM * 2);
          ub[i] = br ? wob : woa;
          us[i] = (unsigned)WS_WO + (((nt * 13) >> 6) * 195 + ks * 5 + nt) * 1024;
          PIN(uo[i][0]); PIN(uo[i][1]); SPIN_(ub[i]); SPIN_(us[i]);
        }
        PIN(gxo); PIN(ggo); PIN(gso);
      }
    }
    dnp += 4 * K1_PS;
    dn = lds_ld(LP(dnp));
    wait_all_gt(dn, 399, dnp);
    if (CLATE == 1 && lw < 2) {
      const unsigned o = (((bi & 3) * 2 + lw) * 64 + lane) * 128;
      touch(pc, o < len ? o : len);
    }
    side_loads();
    const rsrc_t rs = make_rsrc(ws);
    if (lw == 0) {
      __builtin_amdgcn_raw_buffer_store_b64(u32x2{0u, 0u}, rs, (lane >> 5) * 65536 + e * 8, (unsigned)WS_CA + b * 256, 16);
      if (lane < 2) __builtin_amdgcn_raw_buffer_store_b32(0u, rs, lane * 1024, (unsigned)WS_ST + b * 4, 16);
    }
    {
      if (lw) wait_vm<0>(); else wait_vm<2>();
    }
#pragma unroll
    for (int j = 0; j < 5; ++j) asm volatile("" : "+v"(gx[j]), "+v"(gg[j]));
#pragma unroll
    for (int j = 0; j < 8; ++j) asm volatile("" : "+v"(ue[0][j]), "+v"(ue[1][j]));
#pragma unroll
    for (int i = 0; i < 2; ++i)
      if (!i || b < 144)
        st16<K1AUX>(rs, lane * 16, us[i], u32x4{ue[i][0] | ue[i][1] << 16, ue[i][2] | ue[i][3] << 16, ue[i][4] | ue[i][5] << 16, ue[i][6] | ue[i][7] << 16});
    {
      float s = 0.f, s2 = 0.f;
#pragma unroll
      for (int j = 0; j < 5; ++j) { s = dot2(gx[j][0], gg[j][0], s); s2 = dot2(gx[j][1], gg[j][1], s2); }
      s += s2;
      s += __uint_as_float(dpp_u<0xB1>(__float_as_uint(s)));
      s += __uint_as_float(dpp_u<0x4E>(__float_as_uint(s)));
      s += __uint_as_float(dpp_u<0x141>(__float_as_uint(s)));
      s += __uint_as_float(dpp_u<0x140>(__float_as_uint(s)));
      auto sw = __builtin_amdgcn_permlane16_swap(__float_as_uint(s), __float_as_uint(s), false, false);
      s = __uint_as_float(sw[0]) + __uint_as_float(sw[1]);
      if (e == 0) __builtin_amdgcn_raw_buffer_store_b32(__float_as_uint(__builtin_amdgcn_rcpf(1.f + __expf(-s))), rs, gso, 0, 0);
    }
    if (CLATE == 2 && lw < 2) {
      const unsigned o = (((bi & 3) * 2 + lw) * 64 + lane) * 128;
      touch(pc, o < len ? o : len);
    }
  }

}

constexpr int KVT = 2816;
constexpr int K2_UNIT = 4 * KVT;
constexpr int K2_R = 11;
constexpr int K2_W = 5 * K2_UNIT;
constexpr int K2_X = K2_R * K2_UNIT + 8192;
constexpr int K2_C = K2_X + 8 * 3072;
constexpr int K2_LAND = K2_C + 1024;
constexpr int K2_KVF = K2_LAND + 256;
constexpr int K2_DONE = K2_C + 3840;
constexpr int K2_LDS = K2_DONE + 2048;
constexpr int WCAP = 74;
constexpr int L_ARRA = 56, L_ARRB = 60, L_GOA = 64, L_GOB = 68, L_WB = 72, L_AX = 80;
#define HMAP 0
#define BPRIO 3
#define CPRIO 0
#define MPRIO 3
#define THB 6
#define SEND_N 16
#define GO_D 200
#define GO_O0 0
#define GO_O1 0
#define TPOLL 4
#define AWW 3
#define AOW 5
#define BALD 0
#define APRIO 0
#define KSL 10
#define OWIN 8
#define WPRE 24
#define WLAT 13
#define RPRIO 3
#define POLL_SLEEP 2
#define SPIN_TICKS 20000

#define EXP4(P, S, i)                                                                     \
  SBAR();                                                                                 \
  P[i] = __builtin_amdgcn_exp2f(S[i]); P[i + 1] = __builtin_amdgcn_exp2f(S[i + 1]);       \
  P[i + 2] = __builtin_amdgcn_exp2f(S[i + 2]); P[i + 3] = __builtin_amdgcn_exp2f(S[i + 3]); \
  PIN(P); SBAR();

DI void o_voff(int tok0, int br, int h, int lane, unsigned& v0, unsigned& v1) {
  const int tok = tok0 + (lane & 31);
  const unsigned base = (unsigned)WS_O + (tok >> 4) * 40960 + (tok & 15) * 16;
  const int c0 = br * 80 + (h >> 1) * 5, c = c0 + (h & 1) * 2 + (lane >> 5), c4 = c0 + 4;
  v0 = base + c * 256;
  v1 = base + c4 * 256 + (h & 1) * 8;
}
template <bool MK> DI unsigned opk(float lo, float hi) {
  const unsigned u = cvtpk(lo, hi);
  return MK ? (u < 0xfffffffeu ? u : 0xfffffffeu) : u;
}
template <bool MK> DI void store_o(rsrc_t rs, const f32x16& o, float sc, unsigned v0, unsigned v1, unsigned so, int hh) {
  unsigned a0 = opk<MK>(o[0] * sc, o[1] * sc), a1 = opk<MK>(o[2] * sc, o[3] * sc);
  unsigned b0 = opk<MK>(o[4] * sc, o[5] * sc), b1 = opk<MK>(o[6] * sc, o[7] * sc);
  auto s0 = __builtin_amdgcn_permlane32_swap(a0, b0, false, false);
  auto s1 = __builtin_amdgcn_permlane32_swap(a1, b1, false, false);
  __builtin_amdgcn_raw_buffer_store_b128(u32x4{s0[0], s1[0], s0[1], s1[1]}, rs, v0, so, 16);
  if (!hh)
    __builtin_amdgcn_raw_buffer_store_b64(u32x2{opk<MK>(o[8] * sc, o[9] * sc), opk<MK>(o[10] * sc, o[11] * sc)}, rs, v1, so, 16);
}
DI unsigned out_off(int u, int m, int lane) {
  const int g4 = lane >> 4;
  return (((u & 7) * 4 + ((u >> 3) & 3)) * 64 + m * 16 + (lane & 15)) * (DM * 2) + (u >> 5) * 160 + (g4 & 1) * 32 + (g4 >> 1) * 16;
}
DI void store_out(char* out, const f32x4 (&a)[5], unsigned op, int lane) {
  const int g4 = lane >> 4;
#pragma unroll
  for (int t = 0; t < 4; t += 2) {
    auto s0 = __builtin_amdgcn_permlane16_swap(cvtpk(a[t][0], a[t][1]), cvtpk(a[t + 1][0], a[t + 1][1]), false, false);
    auto s1 = __builtin_amdgcn_permlane16_swap(cvtpk(a[t][2], a[t][3]), cvtpk(a[t + 1][2], a[t + 1][3]), false, false);
    *(u32x4*)(out + op + t * 32) = u32x4{s0[0], s1[0], s0[1], s1[1]};
  }
  *(u32x2*)(out + (op - (g4 & 1) * 24) + 128) = u32x2{cvtpk(a[4][0], a[4][1]), cvtpk(a[4][2], a[4][3])};
}
DI bool o_bad(const bf16x8& f) {
  const u32x4 u = __builtin_bit_cast(u32x4, f);
  return __any(umax(umax(u[0], u[1]), umax(u[2], u[3])) == ~0u);
}
DI unsigned o_off(int u, int m, int lane) { return (unsigned)WS_O + (((u & 7) * 4 + ((u >> 3) & 3)) * 4 + m) * 40960 + lane * 16; }
DI unsigned wo_off(int u, int lane) { return (unsigned)WS_WO + (u >> 5) * 204800 + lane * 16; }
DI int wait_tile(char* ws, size_t cnt, int u) {
  const int s = (u & 7) * 32 + (u >> 3);
  const unsigned long long t0 = __builtin_amdgcn_s_memrealtime();
  gu32* c = (gu32*)(ws + cnt + s * 256);
  do {
#pragma unroll 1
    for (int i = 0; i < 8; ++i) {
      if (rfl(__hip_atomic_load(c, RLX_AGENT)) >= 32u) return 1;
      __builtin_amdgcn_s_sleep(POLL_SLEEP);
    }
  } while (__builtin_amdgcn_s_memrealtime() - t0 < SPIN_TICKS);
  gu32* st = (gu32*)(ws + WS_ST + s * 4);
  unsigned v = 0;
  if ((threadIdx.x & 63) == 0) {
    v = __hip_atomic_exchange(st, 1u, RLX_AGENT);
    asm volatile("s_waitcnt vmcnt(0)" : "+v"(v) :: "memory");
    v = __hip_atomic_load((gu32*)(ws + WS_CN + (u & 7) * 128), RLX_AGENT);
    asm volatile("s_waitcnt vmcnt(0)" : "+v"(v) :: "memory");
    if (v >= 32u) v = __hip_atomic_exchange(st, 2u, RLX_AGENT) == 1u;
    else v = 0;
  }
  return rfl(v) ? 1 : 2;
}
DI int lds_arrive(lds_ptr p) {
  int o = 0;
  if ((threadIdx.x & 63) == 0)
    o = __hip_atomic_fetch_add((__attribute__((address_space(3))) int*)p, 1, __ATOMIC_RELAXED, __HIP_MEMORY_SCOPE_WORKGROUP);
  return rfl(o);
}
DI int lds_wait_nz(lds_ptr p) {
  int v;
  while ((v = rfl(lds_ld(p))) == 0) __builtin_amdgcn_s_sleep(1);
  asm volatile("" ::: "memory");
  return v;
}
DI s16x4 bias_slots(float m) {
  const unsigned hi = __float_as_uint(m) & 0xffff0000u;
  const float r1 = m - __uint_as_float(hi);
  const unsigned mi = __float_as_uint(r1) & 0xffff0000u;
  const float r2 = r1 - __uint_as_float(mi);
  const unsigned lo = __float_as_uint(r2) & 0xffff0000u;
  u32x2 v = {((hi >> 16) | mi) ^ 0x80008000u, (lo >> 16) ^ 0x8000u};
  return __builtin_bit_cast(s16x4, v);
}
DI unsigned v_src(unsigned g, unsigned col) { return 5120 + col * 16 + (col >> 4) * 768 + g * 256; }
DI unsigned unit_voff(int piece, int lane, int j) {
  const unsigned c = piece * 64 + lane, ti = c / 176u, ci = c - ti * 176, i = ci - 96, g = __umul24(i, 205) >> 12;
  return (ci < 96 ? ci * 16 + (j >> 1) * 2560 + (j & 1) * 1024 : v_src(g, i - g * 20 + j * 20)) + (ti & 1) * REC + (ti >> 1) * (32 * REC);
}

constexpr int w_reads(int f) {
  int j = 0;
  for (int i = 0; i < f; ++i)
    if (j + WPRE < 100 && j * (100 - WLAT) <= i * (100 - WPRE)) ++j;
  return j;
}

__global__ __launch_bounds__(768, 3) void k2_attn(char* __restrict__ ws, char* __restrict__ out) {
  extern __shared__ __attribute__((aligned(16))) char smem[];
  const lds_ptr lds = (lds_ptr)smem;
  const int tid = threadIdx.x, lane = tid & 63, w = rfl(tid >> 6);
  const int b = blockIdx.x, qb = (b >> 3) & 7, h = (HMAP ? (b & 1) * 16 + (b & 6) * 2 : (b & 7) * 4) + (b >> 6);
  const int r32 = lane & 31, hh = lane >> 5, par = h & 1;
  const char* kvg = ws + WS_KV + (size_t)(h >> 2) * 64 * REC;
  const unsigned lds0 = (unsigned)(size_t)smem, ring0 = lds0;
  const unsigned l16 = lane * 16;
  const rsrc_t rs = make_rsrc(ws);
  const int koff0 = par ? 512 : 0, koff1 = par ? 8 : 1024, voff = 1536, hj = h & 3;
  const u32x4 ones4 = {0x3f803f80u, 0x3f803f80u, 0x3f803f80u, 0x3f803f80u};
  if (w >= 8) {
    if (w == 10) {
      __builtin_amdgcn_s_setprio(3);
      unsigned vo[11];
#pragma unroll
      for (int p = 0; p < 11; ++p) vo[p] = unit_voff(p, lane, hj);
      auto issue = [&](int u) {
        const char* src = kvg + (size_t)(2 * u) * REC;
        const unsigned dst = ring0 + (unsigned)(u < K2_R ? u : u - K2_R) * K2_UNIT;
#pragma unroll
        for (int p = 0; p < 11; ++p) glds16(vo[p], src, dst + p * 1024);
      };
      if (lane < 32) lds_st(lds + K2_LAND + lane * 4, lane < 3 ? 2 : 0);
      lds_bar();
      issue(2);
      issue(3);
      int pub = 2;
      auto publish = [&](int p) {
        lds_st(lds + K2_LAND, p < K2_R ? p : K2_R); lds_st(lds + K2_LAND + 4, p < SEND_N ? p : SEND_N - 1); lds_st(lds + K2_LAND + 8, p < 16 ? p : 17);
      };
#pragma unroll 1
      for (int u = 4; u < 16; ++u) {
        if (u >= K2_R && !__all(lds_ld(lds + K2_DONE + (lane & 7) * 256) >= u - K2_R + 1)) {
          wait_vm<11>();
          if (pub < u - 1) { pub = u - 1; publish(pub); }
          wait_vm<0>();
          pub = u; publish(pub);
          while (!__all(lds_ld(lds + K2_DONE + (lane & 7) * 256) >= u - K2_R + 1)) __builtin_amdgcn_s_sleep(2);
        }
        issue(u);
        wait_vm<22>();
        if (pub < u - 1) { pub = u - 1; publish(pub); }
      }
      wait_vm<11>();
      publish(15);
      wait_vm<0>();
      publish(16);
    } else {
      const int pc = w == 11 ? 10 : w;
      const unsigned vo = unit_voff(pc, lane, hj);
      glds16(vo, kvg, ring0 + pc * 1024);
      glds16(vo, kvg + 2 * REC, ring0 + K2_UNIT + pc * 1024);
      if (w == 11) {
        *(u32x4*)(smem + K2_C + ((lane >> 4) & 1) * 640 + (lane >> 5) * KVT + (lane & 15) * 16) = (lane & 15) == 8 ? ones4 : u32x4{0, 0, 0, 0};
#pragma unroll
        for (int i = 0; i < 8; ++i) *(int*)(smem + K2_DONE + i * 256 + lane * 4) = lane == 1 ? 2 - (i >> 2) - BALD : 0;
#pragma unroll
        for (int i = 0; i < 6; ++i) *(int*)(smem + K2_KVF + i * 256 + lane * 4) = 0;
      }
      asm volatile("s_waitcnt vmcnt(0) lgkmcnt(0)\n\ts_barrier" ::: "memory");
      if (w < 10) {
        __builtin_amdgcn_s_setprio(RPRIO);
        __builtin_amdgcn_s_sleep(KSL + 10);
        bf16x8 p0[4]; u32x2 p1[4]; float g[4];
        const int qt = qb * 8 + (w - 8) * 4;
        const unsigned pp = (unsigned)WS_PQ + ((h >> 2) * 64 + qt) * QREC + (hj >> 1) * 2560;
#pragma unroll
        for (int t = 0; t < 4; ++t) {
          p0[t] = ld16<0>(rs, l16, pp + par * 1536 + t * QREC);
          p1[t] = __builtin_amdgcn_raw_buffer_load_b64(rs, r32 * 16, pp + 1024 + par * 8 + t * QREC, 0);
          g[t] = __uint_as_float(__builtin_amdgcn_raw_buffer_load_b32(rs, r32 * 4, (unsigned)WS_G + (qt + t) * 128, 0));
        }
        const int fr = (lane & 27) == 24 ? (lane & 32) + 20 : lane;
        lds_wait_nz(lds + K2_LAND + 48);
        const bf16x8 f0 = lds_r128(lds + K2_KVF + fr * 16);
        const s16x4 f1 = lds_r64(lds + K2_KVF + 1024 + fr * 8);
        const f32x16 z = {};
        unsigned v0, v1;
        o_voff(qt * 32, 0, h, lane, v0, v1);
#pragma unroll
        for (int t = 0; t < 4; ++t) {
          f32x16 d = MFMA32(f0, p0[t], z);
          d = MFMA32K8(f1, __builtin_bit_cast(s16x4, p1[t]), d);
          store_o<false>(rs, d, g[t] * __builtin_amdgcn_rcpf(d[12] + 1e-6f), v0, v1, t * 81920, hh);
        }
        wait_vm<0>();
        if (lds_arrive(lds + K2_LAND + L_ARRA) == 1 && lane < 32)
          __hip_atomic_fetch_add((gu32*)(ws + WS_CA + (qb * 32 + lane) * 256), 1u, RLX_AGENT);
      } else {
        __builtin_amdgcn_s_setprio(RPRIO);
        __builtin_amdgcn_s_sleep(KSL);
        {
          const unsigned kp = (unsigned)WS_KVP + (h >> 1) * 65536;
          const unsigned c16 = lane & 15, G = lane >> 3 < 4 ? lane >> 3 : 4, i4 = (lane & 7) < 4 ? lane & 7 : 4;
          const unsigned a0 = par * 1024 + l16;
          const unsigned a1 = 2048 + (lane < 48 ? G * 256 + (i4 < 4 ? 8 + 4 * par + i4 : par) * 16 : 1536 + c16 * 16) + (G == 4 ? par * 256 : 0);
          f32x4 s0 = __builtin_bit_cast(f32x4, ld16<0>(rs, a0, kp)), s1 = __builtin_bit_cast(f32x4, ld16<0>(rs, a1, kp));
#pragma unroll
          for (int i = 1; i < 16; ++i) {
            s0 += __builtin_bit_cast(f32x4, ld16<0>(rs, a0, kp + i * 4096));
            s1 += __builtin_bit_cast(f32x4, ld16<0>(rs, a1, kp + i * 4096));
          }
#pragma unroll
          for (int i = 0; i < 2; ++i) {
            const unsigned e = (i && lane < 48) ? (i4 < 4 ? (par ? i4 : 16 + i4) : 20) : c16 + 4 * par, dg = i ? G : lane >> 4;
            const unsigned off = dg < 4 ? ((dg >> 1) * 32 + e) * 16 + (dg & 1) * 8 : 1024 + e * 8;
            const f32x4 sv = i ? s1 : s0;
            *(u32x2*)(smem + K2_KVF + off) = u32x2{cvtpk(sv[0], sv[1]), cvtpk(sv[2], sv[3])};
          }
          wait_lgkm();
          lds_st(lds + K2_LAND + 48, 1);
        }
      }
    }
    const int m = w - 8;
    unsigned oo = o_off(b, m, lane), wo = wo_off(b, lane);
    PIN(oo); PIN(wo);
    f32x4 acc[5];
#pragma unroll
    for (int n = 0; n < 5; ++n) acc[n] = f32x4{0, 0, 0, 0};
    int go;
    constexpr int NPW = (103 - WPRE) / 4;
    {
      const int kq = m & 1;
      unsigned wa = wo + kq * 51200, o0 = oo + kq * 10240, o1 = o0 + 40960 - kq * 81920;
      PIN(wa); PIN(o0); PIN(o1);
      f32x4 snd[5];
#pragma unroll
      for (int n = 0; n < 5; ++n) snd[n] = f32x4{0, 0, 0, 0};
      bf16x8 wf[AWW][5], of[AOW][2];
#pragma unroll
      for (int k = 0; k < AWW; ++k)
#pragma unroll
        for (int n = 0; n < 5; ++n) wf[k][n] = ldf<0>(rs, wa, k * 5 + n);
      SBAR();
      if (w == 11) { go = wait_tile(ws, WS_CA, b); lds_st(lds + K2_LAND + L_GOA, go); }
      else { while ((go = rfl(lds_ld(lds + K2_LAND + L_GOA))) == 0) __builtin_amdgcn_s_sleep(4); asm volatile("" ::: "memory"); }

#pragma unroll
      for (int k = 0; k < AOW; ++k) { of[k][0] = ldf<16>(rs, o0, k); of[k][1] = ldf<16>(rs, o1, k); }
      SBAR();
      __builtin_amdgcn_s_setprio(APRIO);
      SBAR();
#pragma unroll
      for (int k = 0; k < 10; ++k) {
#pragma unroll
        for (int n = 0; n < 5; ++n) {
          acc[n] = MFMA16(wf[k % AWW][n], of[k % AOW][0], acc[n]);
          snd[n] = MFMA16(wf[k % AWW][n], of[k % AOW][1], snd[n]);
        }
        SBAR();
        if (k + AOW < 10) { of[k % AOW][0] = ldf<16>(rs, o0, k + AOW); of[k % AOW][1] = ldf<16>(rs, o1, k + AOW); }
        if (k + AWW < 10) {
#pragma unroll
          for (int n = 0; n < 5; ++n) wf[k % AWW][n] = ldf<0>(rs, wa, (k + AWW) * 5 + n);
        }
        SBAR();
      }
      __builtin_amdgcn_s_setprio(BPRIO);
      while (!__all(lds_ld(lds + K2_DONE + (lane & 7) * 256) >= K2_R)) __builtin_amdgcn_s_sleep(8);
      asm volatile("" ::: "memory");
      f32x4* xs = (f32x4*)(smem + K2_W + (m ^ 1) * (NPW * 1024)) + lane;
#pragma unroll
      for (int n = 0; n < 5; ++n) xs[n * 64] = snd[n];
      wait_lgkm();
      lds_st(lds + K2_LAND + L_AX + m * 4, 1);
    }
    bf16x8 wf[WPRE], of[OWIN];
    unsigned wl = lds0 + K2_W - WPRE * 1024 + l16, wh = wl + 65536;
    PIN(wl); PIN(wh);
#pragma unroll
    for (int f = 0; f < WPRE; ++f) wf[f] = ldf<0>(rs, wo, 100 + f);
    SBAR();
    {
      const char* src = ws + WS_WO + (b >> 5) * 204800 + (100 + WPRE + m * NPW) * 1024;
      const unsigned dst = lds0 + K2_W + m * (NPW * 1024);
#pragma unroll
      for (int p = 5; p < NPW; ++p)
        if (3 * NPW + p < 100 - WPRE || m < 3) {
          if (p >= THB + 5) wait_vm<THB - 1>();
          if (3 * NPW + p == WCAP && m == 3)
            while (!__all(lds_ld(lds + K2_DONE + (lane & 7) * 256) >= K2_R + 2)) __builtin_amdgcn_s_sleep(4);
          glds16(l16, src + p * 1024, 3 * NPW + p >= WCAP && m == 3 ? lds0 + (3 * NPW + p - WCAP) * 1024 : dst + p * 1024);
        }
      lds_wait_nz(lds + K2_LAND + L_AX + (m ^ 1) * 4);
      const f32x4* xr = (const f32x4*)(smem + K2_W + m * (NPW * 1024)) + lane;
      f32x4 t[5];
#pragma unroll
      for (int n = 0; n < 5; ++n) t[n] = xr[n * 64];
      SBAR();
      wait_lgkm();
#pragma unroll
      for (int p = 0; p < 5; ++p) {
        wait_vm<THB - 1>();
        glds16(l16, src + p * 1024, dst + p * 1024);
      }
      SBAR();
#pragma unroll
      for (int n = 0; n < 5; ++n) { acc[n] += t[n]; PIN(acc[n]); }
      SBAR();
    }
    wait_vm<0>();
    lds_arrive(lds + K2_LAND + L_WB);
    while (rfl(lds_ld(lds + K2_LAND + L_WB)) < 4) __builtin_amdgcn_s_sleep(4);
    asm volatile("" ::: "memory");
    __builtin_amdgcn_s_setprio(CPRIO);
    rsrc_t rq = rs;
    unsigned so[20];
#pragma unroll
    for (int i = 0; i < 20; ++i) { so[i] = (i + 20) * 1024; SPIN_(so[i]); }
    SPIN_(rq);
    unsigned op = out_off(b, m, lane);
    PIN(op);
    if (go == 1) {
      unsigned tgt;
      if (w == 8) {
        __builtin_amdgcn_s_setprio(3);
        const int c0 = (lane >> 2) * 5 + (lane & 2), k0 = c0 >> 2;
        const int off = k0 < OWIN ? 0 : 2 * (GO_O0 + (k0 - OWIN) * GO_O1);
        gu32* c = (gu32*)(ws + WS_CB + ((b & 7) * 32 + (b >> 3)) * 256 + lane * 4);
        while (!__all(lds_ld(lds + K2_DONE + (lane & 7) * 256) >= 15)) __builtin_amdgcn_s_sleep(4);
        unsigned v;
        while (!__all((v = __hip_atomic_load(c, RLX_AGENT)) != 0u)) __builtin_amdgcn_s_sleep(TPOLL);
        const unsigned now = (unsigned)__builtin_amdgcn_s_memrealtime();
        v = now - v;
        v = v < 0x100000u ? v : 0x100000u;
        v += dpp_u<0xB1>(v) + off;
        v = umin(v, dpp_u<0x4E>(v)); v = umin(v, dpp_u<0x141>(v)); v = umin(v, dpp_u<0x140>(v));
        const unsigned age = umin(umin(__builtin_amdgcn_readlane(v, 0), __builtin_amdgcn_readlane(v, 16)),
                                  umin(__builtin_amdgcn_readlane(v, 32), __builtin_amdgcn_readlane(v, 48)));
        tgt = (now - (age >> 1) + GO_D) | 1;
        lds_st(lds + K2_LAND + L_GOB, tgt);
      } else {
        while ((tgt = rfl(lds_ld(lds + K2_LAND + L_GOB))) == 0) __builtin_amdgcn_s_sleep(2);
      }
      __builtin_amdgcn_s_setprio(3);
      while ((int)((unsigned)__builtin_amdgcn_s_memrealtime() - tgt) < 0) __builtin_amdgcn_s_sleep(1);
      asm volatile("" ::: "memory");
    }
    if (go == 1) {
      auto ld = [&](int k) { return ld16<16>(rq, oo, so[k]); };
      auto bad = [&](int k) {
        unsigned t = 0;
#pragma unroll
        for (int i = k + 1; i >= k; --i) {
          const u32x4 u = __builtin_bit_cast(u32x4, of[i % OWIN]);
          t = umax(umax(umax(umax(t, u[0]), u[1]), u[2]), u[3]);
        }
        return __any(t == ~0u);
      };
#pragma unroll
      for (int k = 0; k < OWIN; ++k) of[k] = ld(k);
      __builtin_amdgcn_s_setprio(MPRIO);
      SBAR();
      bool nb = bad(0);
      unsigned t = 0;
      auto step = [&](auto F) {
        constexpr int f = F, k = f / 5, n = f % 5;
        if constexpr (n == 0) {
          if (k % 2 == 0 && __builtin_expect(nb, 0)) {
#pragma unroll 1
            do {
#pragma unroll
              for (int i = k; i < k + 2; ++i) of[i % OWIN] = ld(i);
              __builtin_amdgcn_s_waitcnt(0x0f70);
            } while (bad(k));
          }
          constexpr int left = w_reads(f) + WPRE - f - 5;
          if (f + 5 > WPRE) __builtin_amdgcn_s_waitcnt(0xc07f | (left < 15 ? left : 15) << 8);
          t = 0;
        }
        asm volatile("v_mfma_f32_16x16x32_bf16 %0, %1, %2, %0" : "+v"(acc[n]) : "v"(wf[f % WPRE]), "v"(of[k % OWIN]));
        if constexpr (w_reads(f + 1) > w_reads(f)) {
          constexpr int r = w_reads(f) + WPRE;
          wf[r % WPRE] = lds_r128(r < 64 ? LP(wl) + r * 1024 : r < WPRE + WCAP ? LP(wh) + (r - 64) * 1024 : lds + l16 + (r - WPRE - WCAP) * 1024);
        }
        if constexpr (k % 2 == 1 && k < 19) {
          if constexpr (n >= 2 && n < 4) {
            const u32x4 u = __builtin_bit_cast(u32x4, of[(k + 4 - n) % OWIN]);
            t = umax(umax(umax(umax(t, u[0]), u[1]), u[2]), u[3]);
          }
          if (n == 3) nb = __any(t == ~0u);
        }
        if constexpr (n == 4 && k + OWIN < 20) of[k % OWIN] = ld(k + OWIN);
        SBAR();
      };
      static_for<100>(step);
      asm volatile("s_nop 7" : "+v"(acc[0]), "+v"(acc[1]), "+v"(acc[2]), "+v"(acc[3]), "+v"(acc[4]));
      store_out(out, acc, op, lane);
    }
    return;
  }
  __builtin_amdgcn_s_setprio(1);
  const int qs = w & 3, kh = w >> 2;
  const int qt0 = qb * 8 + qs * 2;
  bf16x8 qa0, qb0; s16x4 qa1, qb1;
  const unsigned qp = (unsigned)WS_QB + ((h >> 2) * 64 + qt0) * QREC + (hj >> 1) * 2560;
  const u32x4 km = __builtin_amdgcn_raw_buffer_load_b128(rs, lane < 40 ? l16 : 624u, (unsigned)WS_KMAX + h * 640, 0);
  qa0 = ld16<0>(rs, l16, qp + par * 1536);
  qb0 = ld16<0>(rs, l16, qp + par * 1536 + QREC);
  u32x2 a = __builtin_amdgcn_raw_buffer_load_b64(rs, r32 * 16, qp + 1024 + par * 8, 0);
  u32x2 c = __builtin_amdgcn_raw_buffer_load_b64(rs, r32 * 16, qp + 1024 + par * 8 + QREC, 0);
  const unsigned ga = __builtin_amdgcn_raw_buffer_load_b32(rs, r32 * 4, (unsigned)WS_G + qt0 * 128, 0);
  const unsigned gb = __builtin_amdgcn_raw_buffer_load_b32(rs, r32 * 4, (unsigned)WS_G + qt0 * 128 + 128, 0);
  SBAR();
  {
    const unsigned vo = unit_voff(w, lane, hj);
    glds16(vo, kvg, ring0 + w * 1024);
    glds16(vo, kvg + 2 * REC, ring0 + K2_UNIT + w * 1024);
  }
  SBAR();
  unsigned k0base = ring0 + kh * 2 * KVT + koff0 + l16;
  unsigned k1base = lds0 + (hh ? K2_C + 128 : kh * 2 * KVT + koff1 + r32 * 16), k1mul = hh ? 0 : 1;
  const int cslot = r32 == 20 ? 8 : (r32 < 28 ? 9 : (hh ? 0 : 12));
  unsigned vbase = lds0 + (r32 >= 20 ? K2_C + cslot * 16 : kh * 2 * KVT + voff + hh * 320 + r32 * 16), vmul = r32 >= 20 ? 0 : 1;
  k1mul *= K2_UNIT; vmul *= K2_UNIT;
  unsigned landp = lds0 + (lane == 1 ? K2_DONE + (w ^ 4) * 256 + 4 : K2_LAND + (lane == 2 ? 8 : 0)), donep = lds0 + K2_DONE + w * 256 + lane * 4, one = 1;
  PIN(k0base); PIN(k1base); PIN(k1mul); PIN(vbase); PIN(vmul); PIN(landp); PIN(donep); PIN(one);
  int idx = 0xDDDD; PIN(idx);
  const f32x16 z = {};
  f32x16 oa, ob, sa, sb, pa, pb;
  bf16x8 k0c, k0n, v0c, v1c, v0p, v1p; s16x4 k1c, k1n;
#pragma unroll
  for (int i = 0; i < 16; ++i) { oa[i] = 0.f; ob[i] = 0.f; pb[i] = 0.f; }
  v0p = __builtin_bit_cast(bf16x8, ones4); v1p = v0p;
  PIN(oa); PIN(ob); PIN(pb); PIN(v0p); PIN(v1p);
  unsigned k0a = k0base, k1a = k1base, va = vbase;
  PIN(k0a); PIN(k1a); PIN(va);
  SBAR();
  {
    unsigned e = umax(umax(km[0], km[1]), umax(km[2], km[3]));
    e = umax(e, dpp_u<0xB1>(e)); e = umax(e, dpp_u<0x4E>(e)); e = umax(e, dpp_u<0x141>(e));
    const float kmax2 = ((__uint_as_float(__builtin_amdgcn_readlane(e, 0)) + __uint_as_float(__builtin_amdgcn_readlane(e, 8))) +
                         (__uint_as_float(__builtin_amdgcn_readlane(e, 16)) + __uint_as_float(__builtin_amdgcn_readlane(e, 24)))) +
                        __uint_as_float(__builtin_amdgcn_readlane(e, 32));
    if (hh) { a = u32x2{0, 0}; c = u32x2{0, 0}; }
    const u32x4 ua = __builtin_bit_cast(u32x4, qa0), ub = __builtin_bit_cast(u32x4, qb0);
    float na = dot2(a[0], a[0], dot2(a[1], a[1], 0.f)), nb = dot2(c[0], c[0], dot2(c[1], c[1], 0.f));
#pragma unroll
    for (int j = 0; j < 4; ++j) { na = dot2(ua[j], ua[j], na); nb = dot2(ub[j], ub[j], nb); }
    auto sw = __builtin_amdgcn_permlane32_swap(__float_as_uint(na), __float_as_uint(nb), false, false);
    const float mm = __builtin_amdgcn_sqrtf((__uint_as_float(sw[0]) + __uint_as_float(sw[1])) * kmax2);
    const unsigned nm = (__float_as_uint(mm) >> 16) | 0x8000u;
    auto sm = __builtin_amdgcn_permlane32_swap(nm, nm, false, false);
    if (hh) { a[0] = sm[0]; c[0] = sm[1]; }
    qa1 = __builtin_bit_cast(s16x4, a);
    qb1 = __builtin_bit_cast(s16x4, c);
  }
  float gate = 1.f - __uint_as_float(kh ? gb : ga);
  PIN(qa1); PIN(qb1); PIN(gate);
  SBAR();
  asm volatile("s_waitcnt vmcnt(0) lgkmcnt(0)\n\ts_barrier" ::: "memory");
  lds_ptr k0p = LP(k0a), k1p = LP(k1a), vp = LP(va);
  k0c = lds_r128(k0p); k1c = lds_r64(k1p);
  sa = MFMA32(k0c, qa0, z); sa = MFMA32K8(k1c, qa1, sa);

#define PV(o, v, p, half) o = SMFMA32(v, half ? hi8(p) : lo8(p), o, idx);
#define QK1(s, k, q) s = MFMA32(k, q, z);
#define QK2(s, k, q) s = MFMA32K8(k, q, s);
#define STEP(KC0, KC1, KN0, KN1, VP0, VP1, VC0, VC1, HOOK)   \
  QK1(sb, KC0, qb0)     EXP4(pa, sa, 0)              \
  PV(ob, VP0, pb, 0)    EXP4(pa, sa, 4) HOOK         \
  QK2(sb, KC1, qb1)     EXP4(pa, sa, 8)              \
  PV(ob, VP1, pb, 1)    EXP4(pa, sa, 12)             \
  QK1(sa, KN0, qa0)     EXP4(pb, sb, 0)              \
  PV(oa, VC0, pa, 0)    EXP4(pb, sb, 4)              \
  QK2(sa, KN1, qa1)     EXP4(pb, sb, 8)              \
  PV(oa, VC1, pa, 1)    EXP4(pb, sb, 12)
  const char* stp = ws + WS_CB + (qb * 32 + qs) * 256 + h * 8 + kh * 4;
  SPIN_(stp);
  SBAR();
#pragma unroll
  for (int i = 0; i < 6; ++i) __builtin_amdgcn_s_setprio(1);
  SBAR();
#define SEND
#define WM __builtin_amdgcn_s_waitcnt(0xc07f);
#define STEP2(KC0, KC1, KN0, KN1, VP0, VP1, VC0, VC1)   \
  QK1(sb, KC0, qb0)     EXP4(pa, sa, 0)              \
  PV(ob, VP0, pb, 0)    EXP4(pa, sa, 4)              \
  QK2(sb, KC1, qb1)     EXP4(pa, sa, 8)              \
  PV(ob, VP1, pb, 1)    EXP4(pa, sa, 12) WM          \
  QK1(sa, KN0, qa0)     EXP4(pb, sb, 0)              \
  PV(oa, VC0, pa, 0)    EXP4(pb, sb, 4)              \
  QK2(sa, KN1, qa1)     EXP4(pb, sb, 8)              \
  PV(oa, VC1, pa, 1)    EXP4(pb, sb, 12)
#pragma unroll 1
  for (int n = 2; n < 18; ++n) {
    k0n = lds_r128(LP(k0a) + KVT); k1n = lds_r64(LP(k1a) + KVT);
    v0c = lds_r128(LP(va)); v1c = lds_r128(LP(va) + 640);
    int landed = lds_ld(LP(landp));
    SBAR();
    STEP2(k0c, k1c, k0n, k1n, v0p, v1p, v0c, v1c)
    v0p = lds_r128(LP(va) + KVT); v1p = lds_r128(LP(va) + KVT + 640);
    SBAR();
    {
      int t; unsigned t0, t1;
      asm volatile("v_cmp_gt_i32 vcc, %8, %0\n\ts_setprio 2\n\ts_cbranch_vccz 1f\n\t"
                   "s_setprio 1\n\ts_bitcmp1_b32 vcc_lo, 0\n\ts_cbranch_scc1 0f\n1:\n"
                   ".subsection 1\n"
                   "0:\n\ts_bitcmp1_b32 vcc_lo, 1\n\ts_cbranch_scc1 5f\n\ts_setprio 2\n"
                   "5:\n\ts_cmp_eq_u32 %8, %12\n\ts_cbranch_scc1 3f\n\ts_cmp_lg_u32 %8, %13\n\ts_cbranch_scc1 2f\n\t"
                   "s_memrealtime vcc\n\tv_mbcnt_lo_u32_b32 %6, -1, 0\n\tv_lshlrev_b32 %6, 10, %6\n\t"
                   "s_mov_b64 exec, 0xff\n\ts_waitcnt lgkmcnt(0)\n\t"
                   "v_or_b32_e64 %7, 1, vcc_lo\n\tglobal_store_dword %6, %7, %9 sc1\n\ts_branch 4f\n"
                   "3:\n\tv_subrev_u32 %2, %14, %2\n\tv_mad_i32_i24 %3, %10, %15, %3\n\tv_mad_i32_i24 %4, %11, %15, %4\n"
                   "4:\n\tv_readlane_b32 %5, %0, 2\n\ts_mov_b64 exec, -7\n\tv_add_u32 %1, 4, %1\n\ts_mov_b64 exec, -1\n\t"
                   "s_cmp_ge_i32 %5, %8\n\ts_cbranch_scc1 1b\n"
                   "2:\n\ts_sleep 1\n\tds_read_b32 %0, %1\n\ts_waitcnt lgkmcnt(0)\n\t"
                   "v_readfirstlane_b32 %5, %0\n\ts_cmp_ge_i32 %5, %8\n\ts_cbranch_scc0 2b\n\ts_branch 1b\n"
                   ".subsection 0"
                   : "+v"(landed), "+v"(landp), "+v"(k0a), "+v"(k1a), "+v"(va), "=&s"(t), "=&v"(t0), "=&v"(t1)
                   : "s"(n), "s"(stp), "v"(k1mul), "v"(vmul), "n"(K2_R + 1), "n"(SEND_N), "n"(K2_R * K2_UNIT), "n"(-K2_R)
                   : "memory", "scc", "vcc");
    }
    k0a += K2_UNIT; k1a += k1mul;
    k0c = lds_r128(LP(k0a)); k1c = lds_r64(LP(k1a));
    SBAR();
    asm volatile("ds_add_u32 %0, %1" :: "v"(donep), "v"(one) : "memory");
    va += vmul;
    SBAR();
    STEP2(k0n, k1n, k0c, k1c, v0c, v1c, v0p, v1p)
  }
  ob = SMFMA32(v0p, lo8(pb), ob, idx);
  ob = SMFMA32(v1p, hi8(pb), ob, idx);
  {
    f32x4* mine = (f32x4*)(smem + K2_X + w * 3072) + lane;
    const f32x16 snd = kh ? oa : ob;
#pragma unroll
    for (int j = 0; j < 3; ++j) mine[j * 64] = f32x4{snd[4 * j], snd[4 * j + 1], snd[4 * j + 2], snd[4 * j + 3]};
    asm volatile("" ::: "memory");
    lds_st(lds + K2_LAND + 16 + w * 4, 1);
    while (lds_ld(lds + K2_LAND + 16 + (w ^ 4) * 4) == 0) __builtin_amdgcn_s_sleep(1);
    asm volatile("" ::: "memory");
    const f32x4* oth = (const f32x4*)(smem + K2_X + (w ^ 4) * 3072) + lane;
    if (kh) { oa = ob; qa0 = qb0; qa1 = qb1; }
#pragma unroll
    for (int j = 0; j < 3; ++j) {
      const f32x4 t = oth[j * 64];
      oa[4 * j] += t[0]; oa[4 * j + 1] += t[1]; oa[4 * j + 2] += t[2]; oa[4 * j + 3] += t[3];
    }
  }
  float l = swap32(oa[8]);
  if (hh) l = oa[8];
  if (__any(!(l >= 8.6736174e-19f && l < 3.0e38f))) {
    const s16x4 one4 = {0x3f80, 0x3f80, 0x3f80, 0x3f80};
    const int vr = v_src(hh, (r32 < 20 ? r32 : 0) + hj * 20), kr = (hj >> 1) * 2560 + par * 1024;
    if (hh) qa1 = s16x4{0, 0, 0, 0};
    float mx = -__builtin_inff();
#pragma unroll 1
    for (int t = 0; t < 64; ++t) {
      const char* kp = kvg + (size_t)t * REC;
      bf16x8 k0 = *(const bf16x8*)(kp + kr + koff0 + lane * 16);
      s16x4 k1 = *(const s16x4*)(kp + kr + koff1 + r32 * 16);
      if (hh) k1 = one4;
      f32x16 s = MFMA32(k0, qa0, z); s = MFMA32K8(k1, qa1, s);
#pragma unroll
      for (int i = 0; i < 16; ++i) mx = fmaxf(mx, s[i]);
    }
    mx = fmaxf(mx, swap32(mx));
    if (hh) qa1 = bias_slots(mx);
#pragma unroll
    for (int i = 0; i < 16; ++i) oa[i] = 0.f;
#pragma unroll 1
    for (int t = 0; t < 64; ++t) {
      const char* kp = kvg + (size_t)t * REC;
      bf16x8 k0 = *(const bf16x8*)(kp + kr + koff0 + lane * 16);
      s16x4 k1 = *(const s16x4*)(kp + kr + koff1 + r32 * 16);
      bf16x8 v0 = *(const bf16x8*)(kp + vr);
      bf16x8 v1 = *(const bf16x8*)(kp + vr + 512);
      if (hh) k1 = one4;
      if (r32 == 20) { v0 = __builtin_bit_cast(bf16x8, ones4); v1 = v0; }
      f32x16 s = MFMA32(k0, qa0, z); s = MFMA32K8(k1, qa1, s);
#pragma unroll
      for (int i = 0; i < 16; ++i) s[i] = __builtin_amdgcn_exp2f(s[i]);
      oa = SMFMA32(v0, lo8(s), oa, idx);
      oa = SMFMA32(v1, hi8(s), oa, idx);
    }
    l = swap32(oa[8]);
    if (hh) l = oa[8];
  }
  {
    unsigned v0, v1;
    o_voff((qt0 + kh) * 32, 1, h, lane, v0, v1);
    store_o<true>(rs, oa, gate * __builtin_amdgcn_rcpf(l), v0, v1, 0, hh);
  }
  wait_vm<0>();
  if (lds_arrive(lds + K2_LAND + L_ARRB) == 7) {
    while (rfl(lds_ld(lds + K2_LAND + L_ARRA)) < 2) __builtin_amdgcn_s_sleep(1);
    asm volatile("" ::: "memory");
    unsigned v = 0;
    if (lane == 0) v = __hip_atomic_fetch_add((gu32*)(ws + WS_CN + qb * 128), 1u, RLX_AGENT);
    asm volatile("s_waitcnt vmcnt(0)" : "+v"(v) :: "memory");
    if (rfl(v) == 31u) {
      gu32* st = (gu32*)(ws + WS_ST + (qb * 32 + r32) * 4);
      v = __hip_atomic_load(st, RLX_AGENT);
      if (v == 1u && !hh) v = __hip_atomic_exchange(st, 2u, RLX_AGENT);
      else v = 0;
      unsigned long long todo = __ballot(v == 1u);
#pragma unroll 1
      while (todo) {
        const int u = __builtin_ctzll(todo) * 8 + qb;
        todo &= todo - 1;
#pragma unroll 1
        for (int m = 0; m < 4; ++m) {
          f32x4 acc[5], lo[5];
#pragma unroll
          for (int n = 0; n < 5; ++n) lo[n] = acc[n] = f32x4{0, 0, 0, 0};
#pragma unroll 1
          for (int k = 0; k < 40; ++k) {
            if (k == 10 || k == 20) {
#pragma unroll
              for (int n = 0; n < 5; ++n) { const f32x4 t = acc[n]; acc[n] = k == 10 ? f32x4{0, 0, 0, 0} : t + lo[n]; lo[n] = t; }
            }
            const bf16x8 of = ld16<16>(rs, o_off(u, m, lane), k * 1024);
#pragma unroll
            for (int n = 0; n < 5; ++n) acc[n] = MFMA16(ld16<0>(rs, wo_off(u, lane), (k * 5 + n) * 1024), of, acc[n]);
          }
          store_out(out, acc, out_off(u, m, lane), lane);
        }
      }
    }
  }

}

extern "C" void kernel_launch(void* const* d_in, const int* in_sizes, int n_in, void* d_out, int out_size,
                              void* d_ws, size_t ws_size, hipStream_t stream) {
  const char* x = (const char*)d_in[0];
  const char* wa = (const char*)d_in[1];
  const char* woa = (const char*)d_in[2];
  const char* wb = (const char*)d_in[4];
  const char* wob = (const char*)d_in[5];
  const char* gw = (const char*)d_in[6];
  char* ws = (char*)d_ws + (size_t)WS_SHIFT;
  (void)hipFuncSetAttribute((const void*)k1_qkv, hipFuncAttributeMaxDynamicSharedMemorySize, K1_LDS);
  (void)hipFuncSetAttribute((const void*)k2_attn, hipFuncAttributeMaxDynamicSharedMemorySize, K2_LDS);
  k1_qkv<<<256, 512, K1_LDS, stream>>>(x, wa, wb, woa, wob, gw, ws);
  k2_attn<<<256, 768, K2_LDS, stream>>>(ws, (char*)d_out);
}
```
